```python
import torch

def _softmax(x, axis=-1):
    return torch.softmax(x, dim=axis)
def _gelu(x):
    return torch.nn.functional.gelu(x, approximate="tanh")
def _silu(x):
    return torch.nn.functional.silu(x)
def _sigmoid(x):
    return torch.sigmoid(x)
def _layernorm(x, axis=-1, eps=1e-5):
    m = x.mean(dim=axis, keepdim=True)
    v = x.var(dim=axis, keepdim=True, unbiased=False)
    return (x - m) / torch.sqrt(v + eps)
def _scatter_add(tgt, idx, src, axis=0):
    return tgt.clone().index_add_(axis, idx, src)
def _segment_sum(src, seg, n):
    out = torch.zeros((n,) + src.shape[1:], dtype=src.dtype)
    return out.index_add_(0, seg, src)
def _pad(x, pw):
    flat = tuple(v for pair in reversed(pw) for v in pair)
    return torch.nn.functional.pad(x, flat)

kernel_name = 'standalone__gqa_softmax__near_ridge'

def setup_inputs():

    _w = (lambda a: (lambda t: t.to(torch.bfloat16) if t.is_floating_point() else t)(torch.as_tensor(a)))
    B, Hq, Hkv, Sq, Sk, D = 8, 32, 32, 256, 2048, 128
    return {
        "q": _w((lambda *s: torch.randn(*s, dtype=torch.bfloat16) * 0.8)(B, Hq, Sq, D)),
        "k": _w((lambda *s: torch.randn(*s, dtype=torch.bfloat16) * 0.8)(B, Hkv, Sk, D)),
        "v": _w((lambda *s: torch.randn(*s, dtype=torch.bfloat16) * 0.05)(B, Hkv, Sk, D)),
    }

def reference(q, k, v):
    B, Hq, Sq, D = q.shape
    Hkv = k.shape[1]
    rep = Hq // Hkv
    k = (lambda x, n, axis: torch.repeat_interleave(x, n, dim=axis))(k, rep, axis=1)
    v = (lambda x, n, axis: torch.repeat_interleave(x, n, dim=axis))(v, rep, axis=1)
    scale = D ** -0.5
    scores = torch.einsum("bhqd,bhkd->bhqk", q, k) * scale
    p = _softmax(scores, axis=-1)
    return torch.einsum("bhqk,bhkd->bhqd", p, v)
```

```cpp
#include <hip/hip_runtime.h>
extern "C" __global__ __launch_bounds__(256, 1)
void attn_fwd_kernel(const char* __restrict__ Q, const char* __restrict__ K, const char* __restrict__ V, char* __restrict__ O, char* __restrict__ WS) {
  extern __shared__ __attribute__((aligned(16))) char lds[];
  const unsigned long long ka = (unsigned long long)__builtin_amdgcn_kernarg_segment_ptr();
  const unsigned wid = __builtin_amdgcn_readfirstlane(threadIdx.x >> 6);
  const unsigned ldsb = __builtin_amdgcn_readfirstlane((unsigned)(size_t)lds);
  const unsigned bh = blockIdx.x;
  asm volatile(
    "; kernel arguments: Q, K, V, O[, WS]; everything that does not need them is computed under the load\n\t"
    "s_load_dwordx2 s[40:41], %0, 8\n\t"
    "s_load_dwordx2 s[48:49], %0, 0\n\t"
    "s_load_dwordx2 s[44:45], %0, 16\n\t"
    "s_load_dwordx2 s[52:53], %0, 24\n\t"
    "s_mov_b32 s68, %2\n\t"
    "s_mov_b32 s69, %3\n\t"
    "s_lshl_b32 s59, %1, 19\n\t"
    "s_lshl_b32 s60, %1, 16\n\t"
    "v_mbcnt_lo_u32_b32 v18, -1, 0\n\t"
    "v_mbcnt_hi_u32_b32 v18, -1, v18\n\t"
    "; K/Q DMA voffsets: s=l>>4, row=(l>>2)&3, c4=l&3: 256 row + 64 s + 16 (c4 ^ f(j)), f=[0,2,3,1]; V: 16 (c4 ^ 2 par)\n\t"
    "v_bfe_u32 v21, v18, 2, 2\n\t"
    "v_lshlrev_b32 v23, 8, v21\n\t"
    "v_lshrrev_b32 v20, 4, v18\n\t"
    "v_lshl_add_u32 v23, v20, 6, v23\n\t"
    "v_and_b32 v22, 3, v18\n\t"
    "v_lshl_add_u32 v6, v22, 4, v23\n\t"
    "v_xor_b32 v7, 32, v6\n\t"
    "v_xor_b32 v188, 48, v6\n\t"
    "v_xor_b32 v189, 16, v6\n\t"
    "v_mov_b32 v8, v6\n\t"
    "v_mov_b32 v190, v7\n\t"
    "s_lshl_b32 s64, s68, 12\n\t"
    "s_add_u32 s56, s64, s69\n\t"
    "s_add_u32 s57, s56, 81920\n\t"
    "s_lshl_b32 s65, s68, 14\n\t"
    "s_cmp_ge_u32 s68, 2\n\t"
    "s_cselect_b32 s66, 49152, 0\n\t"
    "s_add_u32 s66, s66, 49152\n\t"
    "s_add_u32 s66, s66, s65\n\t"
    "s_add_u32 s66, s66, s69\n\t"
    "s_mov_b32 s67, s65\n\t"
    "s_mov_b32 s42, 524288\n\t"
    "s_mov_b32 s43, 0x20000\n\t"
    "s_mov_b32 s50, 65536\n\t"
    "s_mov_b32 s51, 0x20000\n\t"
    "s_mov_b32 s46, 540672\n\t"
    "s_mov_b32 s47, 0x20000\n\t"
    "s_mov_b32 s54, 65536\n\t"
    "s_mov_b32 s55, 0x20000\n\t"
    "s_waitcnt lgkmcnt(0)\n\t"
    "s_add_u32 s40, s40, s59\n\t"
    "s_addc_u32 s41, s41, 0\n\t"
    "s_and_b32 s41, s41, 0xffff\n\t"
    "s_add_u32 s48, s48, s60\n\t"
    "s_addc_u32 s49, s49, 0\n\t"
    "s_and_b32 s49, s49, 0xffff\n\t"
    "s_add_u32 s58, s64, 0\n\t"
    "s_add_u32 m0, s56, 0\n\t"
    "s_nop 0\n\t"
    "buffer_load_dwordx4 v6, s[40:43], s58 offen nt lds\n\t"
    "buffer_load_dwordx4 v7, s[40:43], s58 offen offset:1024 nt lds\n\t"
    "buffer_load_dwordx4 v188, s[40:43], s58 offen offset:2048 nt lds\n\t"
    "buffer_load_dwordx4 v189, s[40:43], s58 offen offset:3072 nt lds\n\t"
    "s_add_u32 s44, s44, s59\n\t"
    "s_addc_u32 s45, s45, 0\n\t"
    "s_and_b32 s45, s45, 0xffff\n\t"
    "s_add_u32 s52, s52, s60\n\t"
    "s_addc_u32 s53, s53, 0\n\t"
    "s_and_b32 s53, s53, 0xffff\n\t"
    "v_mov_b32 v26, 0\n\t"
    "global_load_dword v27, v26, s[44:45]\n\t"
    "global_load_dword v27, v26, s[52:53]\n\t"
    "s_add_u32 m0, s66, 0\n\t"
    "s_nop 0\n\t"
    "buffer_load_dwordx4 v6, s[48:51], s67 offen nt lds\n\t"
    "buffer_load_dwordx4 v7, s[48:51], s67 offen offset:1024 nt lds\n\t"
    "buffer_load_dwordx4 v188, s[48:51], s67 offen offset:2048 nt lds\n\t"
    "buffer_load_dwordx4 v189, s[48:51], s67 offen offset:3072 nt lds\n\t"
    "s_add_u32 s67, s67, 4096\n\t"
    "s_add_u32 m0, s66, 4096\n\t"
    "s_nop 0\n\t"
    "buffer_load_dwordx4 v6, s[48:51], s67 offen nt lds\n\t"
    "buffer_load_dwordx4 v7, s[48:51], s67 offen offset:1024 nt lds\n\t"
    "buffer_load_dwordx4 v188, s[48:51], s67 offen offset:2048 nt lds\n\t"
    "buffer_load_dwordx4 v189, s[48:51], s67 offen offset:3072 nt lds\n\t"
    "s_add_u32 s67, s67, 4096\n\t"
    "s_add_u32 m0, s66, 8192\n\t"
    "s_nop 0\n\t"
    "buffer_load_dwordx4 v6, s[48:51], s67 offen nt lds\n\t"
    "buffer_load_dwordx4 v7, s[48:51], s67 offen offset:1024 nt lds\n\t"
    "buffer_load_dwordx4 v188, s[48:51], s67 offen offset:2048 nt lds\n\t"
    "buffer_load_dwordx4 v189, s[48:51], s67 offen offset:3072 nt lds\n\t"
    "s_add_u32 s67, s67, 4096\n\t"
    "s_add_u32 m0, s66, 12288\n\t"
    "s_nop 0\n\t"
    "buffer_load_dwordx4 v6, s[48:51], s67 offen nt lds\n\t"
    "buffer_load_dwordx4 v7, s[48:51], s67 offen offset:1024 nt lds\n\t"
    "buffer_load_dwordx4 v188, s[48:51], s67 offen offset:2048 nt lds\n\t"
    "buffer_load_dwordx4 v189, s[48:51], s67 offen offset:3072 nt lds\n\t"
    "; V descriptor starts one tile early: the in-loop V pieces use the soffset that has already been advanced\n\t"
    "s_sub_u32 s44, s44, 16384\n\t"
    "s_subb_u32 s45, s45, 0\n\t"
    "s_add_u32 s58, s64, 16384\n\t"
    "s_add_u32 m0, s57, 0\n\t"
    "s_nop 0\n\t"
    "buffer_load_dwordx4 v8, s[44:47], s58 offen nt lds\n\t"
    "buffer_load_dwordx4 v190, s[44:47], s58 offen offset:1024 nt lds\n\t"
    "buffer_load_dwordx4 v8, s[44:47], s58 offen offset:2048 nt lds\n\t"
    "buffer_load_dwordx4 v190, s[44:47], s58 offen offset:3072 nt lds\n\t"
    "s_add_u32 s58, s64, 16384\n\t"
    "s_add_u32 m0, s56, 16384\n\t"
    "s_nop 0\n\t"
    "buffer_load_dwordx4 v6, s[40:43], s58 offen nt lds\n\t"
    "buffer_load_dwordx4 v7, s[40:43], s58 offen offset:1024 nt lds\n\t"
    "buffer_load_dwordx4 v188, s[40:43], s58 offen offset:2048 nt lds\n\t"
    "buffer_load_dwordx4 v189, s[40:43], s58 offen offset:3072 nt lds\n\t"
    "; K read base: r=l&15,g=l>>4: 1024(r>>2) + 64(r&3) + 16(g ^ f(r>>2))\n\t"
    "v_bfe_u32 v21, v18, 2, 2\n\t"
    "v_lshlrev_b32 v3, 10, v21\n\t"
    "v_and_b32 v22, 3, v18\n\t"
    "v_lshl_add_u32 v3, v22, 6, v3\n\t"
    "v_lshrrev_b32 v22, 1, v21\n\t"
    "v_xor_b32 v21, v21, v22\n\t"
    "v_lshrrev_b32 v22, 1, v21\n\t"
    "v_and_b32 v21, 1, v21\n\t"
    "v_lshl_or_b32 v21, v21, 1, v22\n\t"
    "v_xor_b32 v21, v21, v20\n\t"
    "v_lshl_add_u32 v3, v21, 4, v3\n\t"
    "v_add_u32 v3, s69, v3\n\t"
    "; V read bases: g=l>>4,q=(l&15)>>2,pp=l&3: 1024 g + 64 q + 32 (b ^ (g&1)) + 8 pp\n\t"
    "v_lshlrev_b32 v4, 10, v20\n\t"
    "v_bfe_u32 v21, v18, 2, 2\n\t"
    "v_lshl_add_u32 v4, v21, 6, v4\n\t"
    "v_and_b32 v21, 1, v20\n\t"
    "v_lshl_add_u32 v4, v21, 5, v4\n\t"
    "v_and_b32 v21, 3, v18\n\t"
    "v_lshl_add_u32 v4, v21, 3, v4\n\t"
    "v_add_u32 v4, s69, v4\n\t"
    "v_add_u32 v4, 81920, v4\n\t"
    "v_xor_b32 v5, 32, v4\n\t"
    "s_getpc_b64 s[30:31]\n\t"
    "L_pc0_%=:\n\t"
    "s_add_u32 s30, s30, L_rare0sub_%=-L_pc0_%=\n\t"
    "s_addc_u32 s31, s31, 0\n\t"
    "s_getpc_b64 s[32:33]\n\t"
    "L_pc1_%=:\n\t"
    "s_add_u32 s32, s32, L_rare1sub_%=-L_pc1_%=\n\t"
    "s_addc_u32 s33, s33, 0\n\t"
    "; ONES, zero-init O, V frags, P1, L1\n\t"
    "v_mov_b32 v176, 0x3f803f80\n\t"
    "v_mov_b32 v177, 0x3f803f80\n\t"
    "v_mov_b32 v178, 0x3f803f80\n\t"
    "v_mov_b32 v179, 0x3f803f80\n\t"
    "v_mov_b32 v180, 0\n\t"
    "v_mov_b32 v181, 0\n\t"
    "v_mov_b32 v182, 0\n\t"
    "v_mov_b32 v183, 0\n\t"
    "s_nop 1\n\t"
    "v_mfma_f32_32x32x16_bf16 a[0:15], v[180:183], v[180:183], 0\n\t"
    "v_mfma_f32_32x32x16_bf16 a[16:31], v[180:183], v[180:183], 0\n\t"
    "v_mfma_f32_32x32x16_bf16 a[32:47], v[180:183], v[180:183], 0\n\t"
    "v_mfma_f32_32x32x16_bf16 a[48:63], v[180:183], v[180:183], 0\n\t"
    "v_mfma_f32_32x32x16_bf16 a[64:79], v[180:183], v[180:183], 0\n\t"
    "v_mfma_f32_32x32x16_bf16 a[80:95], v[180:183], v[180:183], 0\n\t"
    "v_mfma_f32_32x32x16_bf16 a[96:111], v[180:183], v[180:183], 0\n\t"
    "v_mfma_f32_32x32x16_bf16 a[112:127], v[180:183], v[180:183], 0\n\t"
    "v_mfma_f32_32x32x16_bf16 a[160:175], v[180:183], v[180:183], 0\n\t"
    "v_mfma_f32_32x32x16_bf16 a[176:191], v[180:183], v[180:183], 0\n\t"
    "v_mfma_f32_32x32x16_bf16 v[112:127], v[180:183], v[180:183], 0\n\t"
    "v_mfma_f32_32x32x16_bf16 v[160:175], v[180:183], v[180:183], 0\n\t"
    "s_mov_b32 s63, 0x44800000\n\t"
    "s_mov_b32 s59, 65536\n\t"
    "s_mov_b32 s60, 0\n\t"
    "s_mov_b32 s61, 0\n\t"
    "s_mov_b32 s62, 30\n\t"
    "v_mov_b32 v0, v3\n\t"
    "v_mov_b32 v1, v4\n\t"
    "v_mov_b32 v2, v5\n\t"
    "v_add_u32 v24, s66, v3\n\t"
    "v_subrev_u32 v24, s69, v24\n\t"
    "s_mov_b32 s67, 0x3e0293ee\n\t"
    "; K(0) landed -> barrier -> first-half fragments\n\t"
    "s_waitcnt vmcnt(26)\n\t"
    "s_barrier\n\t"
    "ds_read_b128 a[128:131], v0 offset:0\n\t"
    "ds_read_b128 a[132:135], v0 offset:256\n\t"
    "ds_read_b128 a[136:139], v0 offset:512\n\t"
    "ds_read_b128 a[140:143], v0 offset:768\n\t"
    "ds_read_b128 a[144:147], v0 offset:4096\n\t"
    "ds_read_b128 a[148:151], v0 offset:4352\n\t"
    "ds_read_b128 a[152:155], v0 offset:4608\n\t"
    "ds_read_b128 a[156:159], v0 offset:4864\n\t"
    "; Q groups: wait, read, prescale, QK(0), row max of the previous group\n\t"
    "s_waitcnt vmcnt(20)\n\t"
    "ds_read_b128 v[192:195], v24 offset:0\n\t"
    "ds_read_b128 v[196:199], v24 offset:256\n\t"
    "ds_read_b128 v[200:203], v24 offset:512\n\t"
    "ds_read_b128 v[204:207], v24 offset:768\n\t"
    "s_waitcnt lgkmcnt(0)\n\t"
    "v_lshlrev_b32 v26, 16, v192\n\t"
    "v_and_b32 v27, 0xffff0000, v192\n\t"
    "v_lshlrev_b32 v28, 16, v193\n\t"
    "v_and_b32 v29, 0xffff0000, v193\n\t"
    "v_mul_f32 v26, s67, v26\n\t"
    "v_mul_f32 v27, s67, v27\n\t"
    "v_mul_f32 v28, s67, v28\n\t"
    "v_mul_f32 v29, s67, v29\n\t"
    "v_cvt_pk_bf16_f32 v192, v26, v27\n\t"
    "v_cvt_pk_bf16_f32 v193, v28, v29\n\t"
    "v_lshlrev_b32 v26, 16, v194\n\t"
    "v_and_b32 v27, 0xffff0000, v194\n\t"
    "v_lshlrev_b32 v28, 16, v195\n\t"
    "v_and_b32 v29, 0xffff0000, v195\n\t"
    "v_mul_f32 v26, s67, v26\n\t"
    "v_mul_f32 v27, s67, v27\n\t"
    "v_mul_f32 v28, s67, v28\n\t"
    "v_mul_f32 v29, s67, v29\n\t"
    "v_cvt_pk_bf16_f32 v194, v26, v27\n\t"
    "v_cvt_pk_bf16_f32 v195, v28, v29\n\t"
    "v_lshlrev_b32 v26, 16, v196\n\t"
    "v_and_b32 v27, 0xffff0000, v196\n\t"
    "v_lshlrev_b32 v28, 16, v197\n\t"
    "v_and_b32 v29, 0xffff0000, v197\n\t"
    "v_mul_f32 v26, s67, v26\n\t"
    "v_mul_f32 v27, s67, v27\n\t"
    "v_mul_f32 v28, s67, v28\n\t"
    "v_mul_f32 v29, s67, v29\n\t"
    "v_cvt_pk_bf16_f32 v196, v26, v27\n\t"
    "v_cvt_pk_bf16_f32 v197, v28, v29\n\t"
    "v_lshlrev_b32 v26, 16, v198\n\t"
    "v_and_b32 v27, 0xffff0000, v198\n\t"
    "v_lshlrev_b32 v28, 16, v199\n\t"
    "v_and_b32 v29, 0xffff0000, v199\n\t"
    "v_mul_f32 v26, s67, v26\n\t"
    "v_mul_f32 v27, s67, v27\n\t"
    "v_mul_f32 v28, s67, v28\n\t"
    "v_mul_f32 v29, s67, v29\n\t"
    "v_cvt_pk_bf16_f32 v198, v26, v27\n\t"
    "v_cvt_pk_bf16_f32 v199, v28, v29\n\t"
    "v_lshlrev_b32 v26, 16, v200\n\t"
    "v_and_b32 v27, 0xffff0000, v200\n\t"
    "v_lshlrev_b32 v28, 16, v201\n\t"
    "v_and_b32 v29, 0xffff0000, v201\n\t"
    "v_mul_f32 v26, s67, v26\n\t"
    "v_mul_f32 v27, s67, v27\n\t"
    "v_mul_f32 v28, s67, v28\n\t"
    "v_mul_f32 v29, s67, v29\n\t"
    "v_cvt_pk_bf16_f32 v200, v26, v27\n\t"
    "v_cvt_pk_bf16_f32 v201, v28, v29\n\t"
    "v_lshlrev_b32 v26, 16, v202\n\t"
    "v_and_b32 v27, 0xffff0000, v202\n\t"
    "v_lshlrev_b32 v28, 16, v203\n\t"
    "v_and_b32 v29, 0xffff0000, v203\n\t"
    "v_mul_f32 v26, s67, v26\n\t"
    "v_mul_f32 v27, s67, v27\n\t"
    "v_mul_f32 v28, s67, v28\n\t"
    "v_mul_f32 v29, s67, v29\n\t"
    "v_cvt_pk_bf16_f32 v202, v26, v27\n\t"
    "v_cvt_pk_bf16_f32 v203, v28, v29\n\t"
    "v_lshlrev_b32 v26, 16, v204\n\t"
    "v_and_b32 v27, 0xffff0000, v204\n\t"
    "v_lshlrev_b32 v28, 16, v205\n\t"
    "v_and_b32 v29, 0xffff0000, v205\n\t"
    "v_mul_f32 v26, s67, v26\n\t"
    "v_mul_f32 v27, s67, v27\n\t"
    "v_mul_f32 v28, s67, v28\n\t"
    "v_mul_f32 v29, s67, v29\n\t"
    "v_cvt_pk_bf16_f32 v204, v26, v27\n\t"
    "v_cvt_pk_bf16_f32 v205, v28, v29\n\t"
    "v_lshlrev_b32 v26, 16, v206\n\t"
    "v_and_b32 v27, 0xffff0000, v206\n\t"
    "v_lshlrev_b32 v28, 16, v207\n\t"
    "v_and_b32 v29, 0xffff0000, v207\n\t"
    "v_mul_f32 v26, s67, v26\n\t"
    "v_mul_f32 v27, s67, v27\n\t"
    "v_mul_f32 v28, s67, v28\n\t"
    "v_mul_f32 v29, s67, v29\n\t"
    "v_cvt_pk_bf16_f32 v206, v26, v27\n\t"
    "v_cvt_pk_bf16_f32 v207, v28, v29\n\t"
    "s_nop 1\n\t"
    "v_mfma_f32_16x16x32_bf16 v[32:35], a[128:131], v[192:195], 0\n\t"
    "v_mfma_f32_16x16x32_bf16 v[32:35], a[132:135], v[196:199], v[32:35]\n\t"
    "v_mfma_f32_16x16x32_bf16 v[32:35], a[136:139], v[200:203], v[32:35]\n\t"
    "v_mfma_f32_16x16x32_bf16 v[32:35], a[140:143], v[204:207], v[32:35]\n\t"
    "v_mfma_f32_16x16x32_bf16 v[48:51], a[144:147], v[192:195], 0\n\t"
    "v_mfma_f32_16x16x32_bf16 v[48:51], a[148:151], v[196:199], v[48:51]\n\t"
    "v_mfma_f32_16x16x32_bf16 v[48:51], a[152:155], v[200:203], v[48:51]\n\t"
    "v_mfma_f32_16x16x32_bf16 v[48:51], a[156:159], v[204:207], v[48:51]\n\t"
    "s_waitcnt vmcnt(16)\n\t"
    "ds_read_b128 v[208:211], v24 offset:4096\n\t"
    "ds_read_b128 v[212:215], v24 offset:4352\n\t"
    "ds_read_b128 v[216:219], v24 offset:4608\n\t"
    "ds_read_b128 v[220:223], v24 offset:4864\n\t"
    "s_add_u32 s58, s64, 32768\n\t"
    "s_add_u32 m0, s57, 16384\n\t"
    "s_nop 0\n\t"
    "buffer_load_dwordx4 v8, s[44:47], s58 offen nt lds\n\t"
    "buffer_load_dwordx4 v190, s[44:47], s58 offen offset:1024 nt lds\n\t"
    "buffer_load_dwordx4 v8, s[44:47], s58 offen offset:2048 nt lds\n\t"
    "buffer_load_dwordx4 v190, s[44:47], s58 offen offset:3072 nt lds\n\t"
    "s_add_u32 s58, s64, 32768\n\t"
    "s_add_u32 m0, s56, 32768\n\t"
    "s_nop 0\n\t"
    "buffer_load_dwordx4 v6, s[40:43], s58 offen nt lds\n\t"
    "buffer_load_dwordx4 v7, s[40:43], s58 offen offset:1024 nt lds\n\t"
    "buffer_load_dwordx4 v188, s[40:43], s58 offen offset:2048 nt lds\n\t"
    "buffer_load_dwordx4 v189, s[40:43], s58 offen offset:3072 nt lds\n\t"
    "s_nop 3\n\t"
    "v_max3_f32 v30, v32, v33, v34\n\t"
    "v_max3_f32 v30, v30, v35, v48\n\t"
    "v_max3_f32 v30, v30, v49, v50\n\t"
    "v_max_f32 v30, v30, v51\n\t"
    "v_mov_b32 v31, v30\n\t"
    "s_nop 1\n\t"
    "v_permlane16_swap_b32 v30, v31\n\t"
    "s_nop 1\n\t"
    "v_max_f32 v30, v30, v31\n\t"
    "v_mov_b32 v31, v30\n\t"
    "s_nop 1\n\t"
    "v_permlane32_swap_b32 v30, v31\n\t"
    "s_nop 1\n\t"
    "v_max_f32 v30, v30, v31\n\t"
    "v_add_f32 v30, 0x40e00000, v30\n\t"
    "v_sub_f32 v128, 0, v30\n\t"
    "v_sub_f32 v129, 0, v30\n\t"
    "v_sub_f32 v130, 0, v30\n\t"
    "v_sub_f32 v131, 0, v30\n\t"
    "v_sub_f32 v32, v32, v30\n\t"
    "v_sub_f32 v33, v33, v30\n\t"
    "v_sub_f32 v34, v34, v30\n\t"
    "v_sub_f32 v35, v35, v30\n\t"
    "v_sub_f32 v48, v48, v30\n\t"
    "v_sub_f32 v49, v49, v30\n\t"
    "v_sub_f32 v50, v50, v30\n\t"
    "v_sub_f32 v51, v51, v30\n\t"
    "s_waitcnt lgkmcnt(0)\n\t"
    "v_lshlrev_b32 v26, 16, v208\n\t"
    "v_and_b32 v27, 0xffff0000, v208\n\t"
    "v_lshlrev_b32 v28, 16, v209\n\t"
    "v_and_b32 v29, 0xffff0000, v209\n\t"
    "v_mul_f32 v26, s67, v26\n\t"
    "v_mul_f32 v27, s67, v27\n\t"
    "v_mul_f32 v28, s67, v28\n\t"
    "v_mul_f32 v29, s67, v29\n\t"
    "v_cvt_pk_bf16_f32 v208, v26, v27\n\t"
    "v_cvt_pk_bf16_f32 v209, v28, v29\n\t"
    "v_lshlrev_b32 v26, 16, v210\n\t"
    "v_and_b32 v27, 0xffff0000, v210\n\t"
    "v_lshlrev_b32 v28, 16, v211\n\t"
    "v_and_b32 v29, 0xffff0000, v211\n\t"
    "v_mul_f32 v26, s67, v26\n\t"
    "v_mul_f32 v27, s67, v27\n\t"
    "v_mul_f32 v28, s67, v28\n\t"
    "v_mul_f32 v29, s67, v29\n\t"
    "v_cvt_pk_bf16_f32 v210, v26, v27\n\t"
    "v_cvt_pk_bf16_f32 v211, v28, v29\n\t"
    "v_lshlrev_b32 v26, 16, v212\n\t"
    "v_and_b32 v27, 0xffff0000, v212\n\t"
    "v_lshlrev_b32 v28, 16, v213\n\t"
    "v_and_b32 v29, 0xffff0000, v213\n\t"
    "v_mul_f32 v26, s67, v26\n\t"
    "v_mul_f32 v27, s67, v27\n\t"
    "v_mul_f32 v28, s67, v28\n\t"
    "v_mul_f32 v29, s67, v29\n\t"
    "v_cvt_pk_bf16_f32 v212, v26, v27\n\t"
    "v_cvt_pk_bf16_f32 v213, v28, v29\n\t"
    "v_lshlrev_b32 v26, 16, v214\n\t"
    "v_and_b32 v27, 0xffff0000, v214\n\t"
    "v_lshlrev_b32 v28, 16, v215\n\t"
    "v_and_b32 v29, 0xffff0000, v215\n\t"
    "v_mul_f32 v26, s67, v26\n\t"
    "v_mul_f32 v27, s67, v27\n\t"
    "v_mul_f32 v28, s67, v28\n\t"
    "v_mul_f32 v29, s67, v29\n\t"
    "v_cvt_pk_bf16_f32 v214, v26, v27\n\t"
    "v_cvt_pk_bf16_f32 v215, v28, v29\n\t"
    "v_lshlrev_b32 v26, 16, v216\n\t"
    "v_and_b32 v27, 0xffff0000, v216\n\t"
    "v_lshlrev_b32 v28, 16, v217\n\t"
    "v_and_b32 v29, 0xffff0000, v217\n\t"
    "v_mul_f32 v26, s67, v26\n\t"
    "v_mul_f32 v27, s67, v27\n\t"
    "v_mul_f32 v28, s67, v28\n\t"
    "v_mul_f32 v29, s67, v29\n\t"
    "v_cvt_pk_bf16_f32 v216, v26, v27\n\t"
    "v_cvt_pk_bf16_f32 v217, v28, v29\n\t"
    "v_lshlrev_b32 v26, 16, v218\n\t"
    "v_and_b32 v27, 0xffff0000, v218\n\t"
    "v_lshlrev_b32 v28, 16, v219\n\t"
    "v_and_b32 v29, 0xffff0000, v219\n\t"
    "v_mul_f32 v26, s67, v26\n\t"
    "v_mul_f32 v27, s67, v27\n\t"
    "v_mul_f32 v28, s67, v28\n\t"
    "v_mul_f32 v29, s67, v29\n\t"
    "v_cvt_pk_bf16_f32 v218, v26, v27\n\t"
    "v_cvt_pk_bf16_f32 v219, v28, v29\n\t"
    "v_lshlrev_b32 v26, 16, v220\n\t"
    "v_and_b32 v27, 0xffff0000, v220\n\t"
    "v_lshlrev_b32 v28, 16, v221\n\t"
    "v_and_b32 v29, 0xffff0000, v221\n\t"
    "v_mul_f32 v26, s67, v26\n\t"
    "v_mul_f32 v27, s67, v27\n\t"
    "v_mul_f32 v28, s67, v28\n\t"
    "v_mul_f32 v29, s67, v29\n\t"
    "v_cvt_pk_bf16_f32 v220, v26, v27\n\t"
    "v_cvt_pk_bf16_f32 v221, v28, v29\n\t"
    "v_lshlrev_b32 v26, 16, v222\n\t"
    "v_and_b32 v27, 0xffff0000, v222\n\t"
    "v_lshlrev_b32 v28, 16, v223\n\t"
    "v_and_b32 v29, 0xffff0000, v223\n\t"
    "v_mul_f32 v26, s67, v26\n\t"
    "v_mul_f32 v27, s67, v27\n\t"
    "v_mul_f32 v28, s67, v28\n\t"
    "v_mul_f32 v29, s67, v29\n\t"
    "v_cvt_pk_bf16_f32 v222, v26, v27\n\t"
    "v_cvt_pk_bf16_f32 v223, v28, v29\n\t"
    "s_nop 1\n\t"
    "v_mfma_f32_16x16x32_bf16 v[36:39], a[128:131], v[208:211], 0\n\t"
    "v_mfma_f32_16x16x32_bf16 v[36:39], a[132:135], v[212:215], v[36:39]\n\t"
    "v_mfma_f32_16x16x32_bf16 v[36:39], a[136:139], v[216:219], v[36:39]\n\t"
    "v_mfma_f32_16x16x32_bf16 v[36:39], a[140:143], v[220:223], v[36:39]\n\t"
    "v_mfma_f32_16x16x32_bf16 v[52:55], a[144:147], v[208:211], 0\n\t"
    "v_mfma_f32_16x16x32_bf16 v[52:55], a[148:151], v[212:215], v[52:55]\n\t"
    "v_mfma_f32_16x16x32_bf16 v[52:55], a[152:155], v[216:219], v[52:55]\n\t"
    "v_mfma_f32_16x16x32_bf16 v[52:55], a[156:159], v[220:223], v[52:55]\n\t"
    "s_waitcnt vmcnt(20)\n\t"
    "ds_read_b128 v[224:227], v24 offset:8192\n\t"
    "ds_read_b128 v[228:231], v24 offset:8448\n\t"
    "ds_read_b128 v[232:235], v24 offset:8704\n\t"
    "ds_read_b128 v[236:239], v24 offset:8960\n\t"
    "s_add_u32 s58, s64, 49152\n\t"
    "s_add_u32 m0, s57, 32768\n\t"
    "s_nop 0\n\t"
    "buffer_load_dwordx4 v8, s[44:47], s58 offen nt lds\n\t"
    "buffer_load_dwordx4 v190, s[44:47], s58 offen offset:1024 nt lds\n\t"
    "buffer_load_dwordx4 v8, s[44:47], s58 offen offset:2048 nt lds\n\t"
    "buffer_load_dwordx4 v190, s[44:47], s58 offen offset:3072 nt lds\n\t"
    "s_nop 3\n\t"
    "v_max3_f32 v30, v36, v37, v38\n\t"
    "v_max3_f32 v30, v30, v39, v52\n\t"
    "v_max3_f32 v30, v30, v53, v54\n\t"
    "v_max_f32 v30, v30, v55\n\t"
    "v_mov_b32 v31, v30\n\t"
    "s_nop 1\n\t"
    "v_permlane16_swap_b32 v30, v31\n\t"
    "s_nop 1\n\t"
    "v_max_f32 v30, v30, v31\n\t"
    "v_mov_b32 v31, v30\n\t"
    "s_nop 1\n\t"
    "v_permlane32_swap_b32 v30, v31\n\t"
    "s_nop 1\n\t"
    "v_max_f32 v30, v30, v31\n\t"
    "v_add_f32 v30, 0x40e00000, v30\n\t"
    "v_sub_f32 v132, 0, v30\n\t"
    "v_sub_f32 v133, 0, v30\n\t"
    "v_sub_f32 v134, 0, v30\n\t"
    "v_sub_f32 v135, 0, v30\n\t"
    "v_sub_f32 v36, v36, v30\n\t"
    "v_sub_f32 v37, v37, v30\n\t"
    "v_sub_f32 v38, v38, v30\n\t"
    "v_sub_f32 v39, v39, v30\n\t"
    "v_sub_f32 v52, v52, v30\n\t"
    "v_sub_f32 v53, v53, v30\n\t"
    "v_sub_f32 v54, v54, v30\n\t"
    "v_sub_f32 v55, v55, v30\n\t"
    "s_waitcnt lgkmcnt(0)\n\t"
    "v_lshlrev_b32 v26, 16, v224\n\t"
    "v_and_b32 v27, 0xffff0000, v224\n\t"
    "v_lshlrev_b32 v28, 16, v225\n\t"
    "v_and_b32 v29, 0xffff0000, v225\n\t"
    "v_mul_f32 v26, s67, v26\n\t"
    "v_mul_f32 v27, s67, v27\n\t"
    "v_mul_f32 v28, s67, v28\n\t"
    "v_mul_f32 v29, s67, v29\n\t"
    "v_cvt_pk_bf16_f32 v224, v26, v27\n\t"
    "v_cvt_pk_bf16_f32 v225, v28, v29\n\t"
    "v_lshlrev_b32 v26, 16, v226\n\t"
    "v_and_b32 v27, 0xffff0000, v226\n\t"
    "v_lshlrev_b32 v28, 16, v227\n\t"
    "v_and_b32 v29, 0xffff0000, v227\n\t"
    "v_mul_f32 v26, s67, v26\n\t"
    "v_mul_f32 v27, s67, v27\n\t"
    "v_mul_f32 v28, s67, v28\n\t"
    "v_mul_f32 v29, s67, v29\n\t"
    "v_cvt_pk_bf16_f32 v226, v26, v27\n\t"
    "v_cvt_pk_bf16_f32 v227, v28, v29\n\t"
    "v_lshlrev_b32 v26, 16, v228\n\t"
    "v_and_b32 v27, 0xffff0000, v228\n\t"
    "v_lshlrev_b32 v28, 16, v229\n\t"
    "v_and_b32 v29, 0xffff0000, v229\n\t"
    "v_mul_f32 v26, s67, v26\n\t"
    "v_mul_f32 v27, s67, v27\n\t"
    "v_mul_f32 v28, s67, v28\n\t"
    "v_mul_f32 v29, s67, v29\n\t"
    "v_cvt_pk_bf16_f32 v228, v26, v27\n\t"
    "v_cvt_pk_bf16_f32 v229, v28, v29\n\t"
    "v_lshlrev_b32 v26, 16, v230\n\t"
    "v_and_b32 v27, 0xffff0000, v230\n\t"
    "v_lshlrev_b32 v28, 16, v231\n\t"
    "v_and_b32 v29, 0xffff0000, v231\n\t"
    "v_mul_f32 v26, s67, v26\n\t"
    "v_mul_f32 v27, s67, v27\n\t"
    "v_mul_f32 v28, s67, v28\n\t"
    "v_mul_f32 v29, s67, v29\n\t"
    "v_cvt_pk_bf16_f32 v230, v26, v27\n\t"
    "v_cvt_pk_bf16_f32 v231, v28, v29\n\t"
    "v_lshlrev_b32 v26, 16, v232\n\t"
    "v_and_b32 v27, 0xffff0000, v232\n\t"
    "v_lshlrev_b32 v28, 16, v233\n\t"
    "v_and_b32 v29, 0xffff0000, v233\n\t"
    "v_mul_f32 v26, s67, v26\n\t"
    "v_mul_f32 v27, s67, v27\n\t"
    "v_mul_f32 v28, s67, v28\n\t"
    "v_mul_f32 v29, s67, v29\n\t"
    "v_cvt_pk_bf16_f32 v232, v26, v27\n\t"
    "v_cvt_pk_bf16_f32 v233, v28, v29\n\t"
    "v_lshlrev_b32 v26, 16, v234\n\t"
    "v_and_b32 v27, 0xffff0000, v234\n\t"
    "v_lshlrev_b32 v28, 16, v235\n\t"
    "v_and_b32 v29, 0xffff0000, v235\n\t"
    "v_mul_f32 v26, s67, v26\n\t"
    "v_mul_f32 v27, s67, v27\n\t"
    "v_mul_f32 v28, s67, v28\n\t"
    "v_mul_f32 v29, s67, v29\n\t"
    "v_cvt_pk_bf16_f32 v234, v26, v27\n\t"
    "v_cvt_pk_bf16_f32 v235, v28, v29\n\t"
    "v_lshlrev_b32 v26, 16, v236\n\t"
    "v_and_b32 v27, 0xffff0000, v236\n\t"
    "v_lshlrev_b32 v28, 16, v237\n\t"
    "v_and_b32 v29, 0xffff0000, v237\n\t"
    "v_mul_f32 v26, s67, v26\n\t"
    "v_mul_f32 v27, s67, v27\n\t"
    "v_mul_f32 v28, s67, v28\n\t"
    "v_mul_f32 v29, s67, v29\n\t"
    "v_cvt_pk_bf16_f32 v236, v26, v27\n\t"
    "v_cvt_pk_bf16_f32 v237, v28, v29\n\t"
    "v_lshlrev_b32 v26, 16, v238\n\t"
    "v_and_b32 v27, 0xffff0000, v238\n\t"
    "v_lshlrev_b32 v28, 16, v239\n\t"
    "v_and_b32 v29, 0xffff0000, v239\n\t"
    "v_mul_f32 v26, s67, v26\n\t"
    "v_mul_f32 v27, s67, v27\n\t"
    "v_mul_f32 v28, s67, v28\n\t"
    "v_mul_f32 v29, s67, v29\n\t"
    "v_cvt_pk_bf16_f32 v238, v26, v27\n\t"
    "v_cvt_pk_bf16_f32 v239, v28, v29\n\t"
    "s_nop 1\n\t"
    "v_mfma_f32_16x16x32_bf16 v[40:43], a[128:131], v[224:227], 0\n\t"
    "v_mfma_f32_16x16x32_bf16 v[40:43], a[132:135], v[228:231], v[40:43]\n\t"
    "v_mfma_f32_16x16x32_bf16 v[40:43], a[136:139], v[232:235], v[40:43]\n\t"
    "v_mfma_f32_16x16x32_bf16 v[40:43], a[140:143], v[236:239], v[40:43]\n\t"
    "v_mfma_f32_16x16x32_bf16 v[56:59], a[144:147], v[224:227], 0\n\t"
    "v_mfma_f32_16x16x32_bf16 v[56:59], a[148:151], v[228:231], v[56:59]\n\t"
    "v_mfma_f32_16x16x32_bf16 v[56:59], a[152:155], v[232:235], v[56:59]\n\t"
    "v_mfma_f32_16x16x32_bf16 v[56:59], a[156:159], v[236:239], v[56:59]\n\t"
    "s_waitcnt vmcnt(20)\n\t"
    "ds_read_b128 v[240:243], v24 offset:12288\n\t"
    "ds_read_b128 v[244:247], v24 offset:12544\n\t"
    "ds_read_b128 v[248:251], v24 offset:12800\n\t"
    "ds_read_b128 v[252:255], v24 offset:13056\n\t"
    "s_nop 3\n\t"
    "v_max3_f32 v30, v40, v41, v42\n\t"
    "v_max3_f32 v30, v30, v43, v56\n\t"
    "v_max3_f32 v30, v30, v57, v58\n\t"
    "v_max_f32 v30, v30, v59\n\t"
    "v_mov_b32 v31, v30\n\t"
    "s_nop 1\n\t"
    "v_permlane16_swap_b32 v30, v31\n\t"
    "s_nop 1\n\t"
    "v_max_f32 v30, v30, v31\n\t"
    "v_mov_b32 v31, v30\n\t"
    "s_nop 1\n\t"
    "v_permlane32_swap_b32 v30, v31\n\t"
    "s_nop 1\n\t"
    "v_max_f32 v30, v30, v31\n\t"
    "v_add_f32 v30, 0x40e00000, v30\n\t"
    "v_sub_f32 v136, 0, v30\n\t"
    "v_sub_f32 v137, 0, v30\n\t"
    "v_sub_f32 v138, 0, v30\n\t"
    "v_sub_f32 v139, 0, v30\n\t"
    "v_sub_f32 v40, v40, v30\n\t"
    "v_sub_f32 v41, v41, v30\n\t"
    "v_sub_f32 v42, v42, v30\n\t"
    "v_sub_f32 v43, v43, v30\n\t"
    "v_sub_f32 v56, v56, v30\n\t"
    "v_sub_f32 v57, v57, v30\n\t"
    "v_sub_f32 v58, v58, v30\n\t"
    "v_sub_f32 v59, v59, v30\n\t"
    "s_waitcnt lgkmcnt(0)\n\t"
    "v_lshlrev_b32 v26, 16, v240\n\t"
    "v_and_b32 v27, 0xffff0000, v240\n\t"
    "v_lshlrev_b32 v28, 16, v241\n\t"
    "v_and_b32 v29, 0xffff0000, v241\n\t"
    "v_mul_f32 v26, s67, v26\n\t"
    "v_mul_f32 v27, s67, v27\n\t"
    "v_mul_f32 v28, s67, v28\n\t"
    "v_mul_f32 v29, s67, v29\n\t"
    "v_cvt_pk_bf16_f32 v240, v26, v27\n\t"
    "v_cvt_pk_bf16_f32 v241, v28, v29\n\t"
    "v_lshlrev_b32 v26, 16, v242\n\t"
    "v_and_b32 v27, 0xffff0000, v242\n\t"
    "v_lshlrev_b32 v28, 16, v243\n\t"
    "v_and_b32 v29, 0xffff0000, v243\n\t"
    "v_mul_f32 v26, s67, v26\n\t"
    "v_mul_f32 v27, s67, v27\n\t"
    "v_mul_f32 v28, s67, v28\n\t"
    "v_mul_f32 v29, s67, v29\n\t"
    "v_cvt_pk_bf16_f32 v242, v26, v27\n\t"
    "v_cvt_pk_bf16_f32 v243, v28, v29\n\t"
    "v_lshlrev_b32 v26, 16, v244\n\t"
    "v_and_b32 v27, 0xffff0000, v244\n\t"
    "v_lshlrev_b32 v28, 16, v245\n\t"
    "v_and_b32 v29, 0xffff0000, v245\n\t"
    "v_mul_f32 v26, s67, v26\n\t"
    "v_mul_f32 v27, s67, v27\n\t"
    "v_mul_f32 v28, s67, v28\n\t"
    "v_mul_f32 v29, s67, v29\n\t"
    "v_cvt_pk_bf16_f32 v244, v26, v27\n\t"
    "v_cvt_pk_bf16_f32 v245, v28, v29\n\t"
    "v_lshlrev_b32 v26, 16, v246\n\t"
    "v_and_b32 v27, 0xffff0000, v246\n\t"
    "v_lshlrev_b32 v28, 16, v247\n\t"
    "v_and_b32 v29, 0xffff0000, v247\n\t"
    "v_mul_f32 v26, s67, v26\n\t"
    "v_mul_f32 v27, s67, v27\n\t"
    "v_mul_f32 v28, s67, v28\n\t"
    "v_mul_f32 v29, s67, v29\n\t"
    "v_cvt_pk_bf16_f32 v246, v26, v27\n\t"
    "v_cvt_pk_bf16_f32 v247, v28, v29\n\t"
    "v_lshlrev_b32 v26, 16, v248\n\t"
    "v_and_b32 v27, 0xffff0000, v248\n\t"
    "v_lshlrev_b32 v28, 16, v249\n\t"
    "v_and_b32 v29, 0xffff0000, v249\n\t"
    "v_mul_f32 v26, s67, v26\n\t"
    "v_mul_f32 v27, s67, v27\n\t"
    "v_mul_f32 v28, s67, v28\n\t"
    "v_mul_f32 v29, s67, v29\n\t"
    "v_cvt_pk_bf16_f32 v248, v26, v27\n\t"
    "v_cvt_pk_bf16_f32 v249, v28, v29\n\t"
    "v_lshlrev_b32 v26, 16, v250\n\t"
    "v_and_b32 v27, 0xffff0000, v250\n\t"
    "v_lshlrev_b32 v28, 16, v251\n\t"
    "v_and_b32 v29, 0xffff0000, v251\n\t"
    "v_mul_f32 v26, s67, v26\n\t"
    "v_mul_f32 v27, s67, v27\n\t"
    "v_mul_f32 v28, s67, v28\n\t"
    "v_mul_f32 v29, s67, v29\n\t"
    "v_cvt_pk_bf16_f32 v250, v26, v27\n\t"
    "v_cvt_pk_bf16_f32 v251, v28, v29\n\t"
    "v_lshlrev_b32 v26, 16, v252\n\t"
    "v_and_b32 v27, 0xffff0000, v252\n\t"
    "v_lshlrev_b32 v28, 16, v253\n\t"
    "v_and_b32 v29, 0xffff0000, v253\n\t"
    "v_mul_f32 v26, s67, v26\n\t"
    "v_mul_f32 v27, s67, v27\n\t"
    "v_mul_f32 v28, s67, v28\n\t"
    "v_mul_f32 v29, s67, v29\n\t"
    "v_cvt_pk_bf16_f32 v252, v26, v27\n\t"
    "v_cvt_pk_bf16_f32 v253, v28, v29\n\t"
    "v_lshlrev_b32 v26, 16, v254\n\t"
    "v_and_b32 v27, 0xffff0000, v254\n\t"
    "v_lshlrev_b32 v28, 16, v255\n\t"
    "v_and_b32 v29, 0xffff0000, v255\n\t"
    "v_mul_f32 v26, s67, v26\n\t"
    "v_mul_f32 v27, s67, v27\n\t"
    "v_mul_f32 v28, s67, v28\n\t"
    "v_mul_f32 v29, s67, v29\n\t"
    "v_cvt_pk_bf16_f32 v254, v26, v27\n\t"
    "v_cvt_pk_bf16_f32 v255, v28, v29\n\t"
    "s_nop 1\n\t"
    "v_mfma_f32_16x16x32_bf16 v[44:47], a[128:131], v[240:243], 0\n\t"
    "v_mfma_f32_16x16x32_bf16 v[44:47], a[132:135], v[244:247], v[44:47]\n\t"
    "v_mfma_f32_16x16x32_bf16 v[44:47], a[136:139], v[248:251], v[44:47]\n\t"
    "v_mfma_f32_16x16x32_bf16 v[44:47], a[140:143], v[252:255], v[44:47]\n\t"
    "v_mfma_f32_16x16x32_bf16 v[60:63], a[144:147], v[240:243], 0\n\t"
    "v_mfma_f32_16x16x32_bf16 v[60:63], a[148:151], v[244:247], v[60:63]\n\t"
    "v_mfma_f32_16x16x32_bf16 v[60:63], a[152:155], v[248:251], v[60:63]\n\t"
    "v_mfma_f32_16x16x32_bf16 v[60:63], a[156:159], v[252:255], v[60:63]\n\t"
    "; V(0), K(1) landed, every wave has read its Q -> barrier; K(3), V(3) into the freed Q slots\n\t"
    "s_barrier\n\t"
    "s_mov_b32 s27, 130\n\t"
    "s_memrealtime s[38:39]\n\t"
    "s_waitcnt lgkmcnt(0)\n\t"
    "s_add_u32 s36, s38, 130\n\t"
    "v_mfma_f32_16x16x32_bf16 a[0:3], a[160:163], v[112:115], a[0:3]\n\t"
    "s_add_u32 s58, s64, 49152\n\t"
    "s_add_u32 m0, s56, 49152\n\t"
    "s_nop 0\n\t"
    "v_exp_f32 v10, v32\n\t"
    "v_mfma_f32_16x16x32_bf16 a[16:19], a[164:167], v[112:115], a[16:19]\n\t"
    "buffer_load_dwordx4 v6, s[40:43], s58 offen nt lds\n\t"
    "buffer_load_dwordx4 v7, s[40:43], s58 offen offset:1024 nt lds\n\t"
    "buffer_load_dwordx4 v188, s[40:43], s58 offen offset:2048 nt lds\n\t"
    "v_exp_f32 v11, v33\n\t"
    "v_mfma_f32_16x16x32_bf16 a[32:35], a[168:171], v[112:115], a[32:35]\n\t"
    "buffer_load_dwordx4 v189, s[40:43], s58 offen offset:3072 nt lds\n\t"
    "s_add_u32 s58, s64, 65536\n\t"
    "s_add_u32 m0, s57, 49152\n\t"
    "ds_read_b128 a[128:131], v0 offset:8192\n\t"
    "v_cvt_pk_bf16_f32 v96, v10, v11\n\t"
    "v_mfma_f32_16x16x32_bf16 a[48:51], a[172:175], v[112:115], a[48:51]\n\t"
    "s_nop 0\n\t"
    "buffer_load_dwordx4 v8, s[44:47], s58 offen nt lds\n\t"
    "buffer_load_dwordx4 v190, s[44:47], s58 offen offset:1024 nt lds\n\t"
    "v_exp_f32 v12, v34\n\t"
    "v_mfma_f32_16x16x32_bf16 a[64:67], a[176:179], v[112:115], a[64:67]\n\t"
    "buffer_load_dwordx4 v8, s[44:47], s58 offen offset:2048 nt lds\n\t"
    "buffer_load_dwordx4 v190, s[44:47], s58 offen offset:3072 nt lds\n\t"
    "v_exp_f32 v13, v35\n\t"
    "v_mfma_f32_16x16x32_bf16 a[80:83], a[180:183], v[112:115], a[80:83]\n\t"
    "ds_read_b128 a[132:135], v0 offset:8448\n\t"
    "v_cvt_pk_bf16_f32 v97, v12, v13\n\t"
    "v_mfma_f32_16x16x32_bf16 a[96:99], a[184:187], v[112:115], a[96:99]\n\t"
    "s_add_u32 m0, s59, s56\n\t"
    "s_add_u32 s61, s61, 16384\n\t"
    "v_mfma_f32_16x16x32_bf16 a[112:115], a[188:191], v[112:115], a[112:115]\n\t"
    "v_max3_f32 v30, v44, v45, v46\n\t"
    "v_max3_f32 v30, v30, v47, v60\n\t"
    "v_max3_f32 v30, v30, v61, v62\n\t"
    "v_exp_f32 v14, v48\n\t"
    "v_mfma_f32_16x16x32_bf16 a[4:7], a[160:163], v[116:119], a[4:7]\n\t"
    "v_max_f32 v30, v30, v63\n\t"
    "v_mov_b32 v31, v30\n\t"
    "s_nop 1\n\t"
    "v_exp_f32 v15, v49\n\t"
    "v_mfma_f32_16x16x32_bf16 a[20:23], a[164:167], v[116:119], a[20:23]\n\t"
    "v_permlane16_swap_b32 v30, v31\n\t"
    "s_nop 1\n\t"
    "v_max_f32 v30, v30, v31\n\t"
    "ds_read_b128 a[136:139], v0 offset:8704\n\t"
    "v_cvt_pk_bf16_f32 v98, v14, v15\n\t"
    "v_mfma_f32_16x16x32_bf16 a[36:39], a[168:171], v[116:119], a[36:39]\n\t"
    "v_mov_b32 v31, v30\n\t"
    "s_nop 1\n\t"
    "v_permlane32_swap_b32 v30, v31\n\t"
    "buffer_load_dwordx4 v6, s[40:43], s58 offen nt lds\n\t"
    "v_mfma_f32_16x16x32_bf16 a[52:55], a[172:175], v[116:119], a[52:55]\n\t"
    "s_nop 1\n\t"
    "v_max_f32 v30, v30, v31\n\t"
    "v_add_f32 v30, 0x40e00000, v30\n\t"
    "v_exp_f32 v16, v50\n\t"
    "v_mfma_f32_16x16x32_bf16 a[68:71], a[176:179], v[116:119], a[68:71]\n\t"
    "v_sub_f32 v140, 0, v30\n\t"
    "v_sub_f32 v141, 0, v30\n\t"
    "v_sub_f32 v142, 0, v30\n\t"
    "v_exp_f32 v17, v51\n\t"
    "v_mfma_f32_16x16x32_bf16 a[84:87], a[180:183], v[116:119], a[84:87]\n\t"
    "v_sub_f32 v143, 0, v30\n\t"
    "v_sub_f32 v44, v44, v30\n\t"
    "v_sub_f32 v45, v45, v30\n\t"
    "ds_read_b128 a[140:143], v0 offset:8960\n\t"
    "v_cvt_pk_bf16_f32 v99, v16, v17\n\t"
    "v_mfma_f32_16x16x32_bf16 a[100:103], a[184:187], v[116:119], a[100:103]\n\t"
    "v_sub_f32 v46, v46, v30\n\t"
    "v_sub_f32 v47, v47, v30\n\t"
    "v_sub_f32 v60, v60, v30\n\t"
    "v_exp_f32 v10, v36\n\t"
    "v_mfma_f32_16x16x32_bf16 a[116:119], a[188:191], v[116:119], a[116:119]\n\t"
    "v_sub_f32 v61, v61, v30\n\t"
    "v_sub_f32 v62, v62, v30\n\t"
    "v_sub_f32 v63, v63, v30\n\t"
    "v_exp_f32 v11, v37\n\t"
    "v_mfma_f32_16x16x32_bf16 v[144:147], v[176:179], v[96:99], v[160:163]\n\t"
    "ds_read_b128 a[144:147], v0 offset:12288\n\t"
    "v_cvt_pk_bf16_f32 v100, v10, v11\n\t"
    "s_sleep 2\n\t"
    "v_mfma_f32_16x16x32_bf16 a[8:11], a[160:163], v[120:123], a[8:11]\n\t"
    "v_exp_f32 v12, v38\n\t"
    "v_mfma_f32_16x16x32_bf16 a[24:27], a[164:167], v[120:123], a[24:27]\n\t"
    "v_exp_f32 v13, v39\n\t"
    "v_mfma_f32_16x16x32_bf16 a[40:43], a[168:171], v[120:123], a[40:43]\n\t"
    "ds_read_b128 a[148:151], v0 offset:12544\n\t"
    "v_cvt_pk_bf16_f32 v101, v12, v13\n\t"
    "v_mfma_f32_16x16x32_bf16 a[56:59], a[172:175], v[120:123], a[56:59]\n\t"
    "v_exp_f32 v14, v52\n\t"
    "v_mfma_f32_16x16x32_bf16 a[72:75], a[176:179], v[120:123], a[72:75]\n\t"
    "v_exp_f32 v15, v53\n\t"
    "v_mfma_f32_16x16x32_bf16 a[88:91], a[180:183], v[120:123], a[88:91]\n\t"
    "ds_read_b128 a[152:155], v0 offset:12800\n\t"
    "v_cvt_pk_bf16_f32 v102, v14, v15\n\t"
    "v_mfma_f32_16x16x32_bf16 a[104:107], a[184:187], v[120:123], a[104:107]\n\t"
    "v_exp_f32 v16, v54\n\t"
    "v_mfma_f32_16x16x32_bf16 a[120:123], a[188:191], v[120:123], a[120:123]\n\t"
    "s_waitcnt vmcnt(21)\n\t"
    "s_barrier\n\t"
    "v_exp_f32 v17, v55\n\t"
    "v_mfma_f32_16x16x32_bf16 a[12:15], a[160:163], v[124:127], a[12:15]\n\t"
    "ds_read_b128 a[156:159], v0 offset:13056\n\t"
    "v_cvt_pk_bf16_f32 v103, v16, v17\n\t"
    "v_mfma_f32_16x16x32_bf16 a[28:31], a[164:167], v[124:127], a[28:31]\n\t"
    "buffer_load_dwordx4 v7, s[40:43], s58 offen offset:1024 nt lds\n\t"
    "v_mfma_f32_16x16x32_bf16 a[44:47], a[168:171], v[124:127], a[44:47]\n\t"
    "v_exp_f32 v10, v40\n\t"
    "v_mfma_f32_16x16x32_bf16 v[148:151], v[176:179], v[100:103], v[164:167]\n\t"
    "v_exp_f32 v11, v41\n\t"
    "v_mfma_f32_16x16x32_bf16 a[60:63], a[172:175], v[124:127], a[60:63]\n\t"
    "ds_read_b64_tr_b16 a[160:161], v1 offset:0\n\t"
    "v_cvt_pk_bf16_f32 v104, v10, v11\n\t"
    "v_mfma_f32_16x16x32_bf16 a[76:79], a[176:179], v[124:127], a[76:79]\n\t"
    "v_exp_f32 v12, v42\n\t"
    "v_mfma_f32_16x16x32_bf16 a[92:95], a[180:183], v[124:127], a[92:95]\n\t"
    "v_exp_f32 v13, v43\n\t"
    "v_mfma_f32_16x16x32_bf16 a[108:111], a[184:187], v[124:127], a[108:111]\n\t"
    "ds_read_b64_tr_b16 a[162:163], v1 offset:4096\n\t"
    "v_cvt_pk_bf16_f32 v105, v12, v13\n\t"
    "v_mfma_f32_16x16x32_bf16 a[124:127], a[188:191], v[124:127], a[124:127]\n\t"
    "ds_read_b64_tr_b16 a[164:165], v2 offset:0\n\t"
    "s_waitcnt lgkmcnt(3)\n\t"
    "s_sleep 2\n\t"
    "v_mfma_f32_16x16x32_bf16 v[64:67], a[128:131], v[192:195], v[128:131]\n\t"
    "v_exp_f32 v14, v56\n\t"
    "v_mfma_f32_16x16x32_bf16 v[64:67], a[132:135], v[196:199], v[64:67]\n\t"
    "v_exp_f32 v15, v57\n\t"
    "v_mfma_f32_16x16x32_bf16 v[64:67], a[136:139], v[200:203], v[64:67]\n\t"
    "ds_read_b64_tr_b16 a[166:167], v2 offset:4096\n\t"
    "v_cvt_pk_bf16_f32 v106, v14, v15\n\t"
    "v_mfma_f32_16x16x32_bf16 v[64:67], a[140:143], v[204:207], v[64:67]\n\t"
    "v_exp_f32 v16, v58\n\t"
    "v_mfma_f32_16x16x32_bf16 v[80:83], a[144:147], v[192:195], v[128:131]\n\t"
    "v_exp_f32 v17, v59\n\t"
    "v_mfma_f32_16x16x32_bf16 v[80:83], a[148:151], v[196:199], v[80:83]\n\t"
    "ds_read_b64_tr_b16 a[168:169], v1 offset:256\n\t"
    "v_cvt_pk_bf16_f32 v107, v16, v17\n\t"
    "v_mfma_f32_16x16x32_bf16 v[80:83], a[152:155], v[200:203], v[80:83]\n\t"
    "ds_read_b64_tr_b16 a[170:171], v1 offset:4352\n\t"
    "v_mfma_f32_16x16x32_bf16 v[80:83], a[156:159], v[204:207], v[80:83]\n\t"
    "v_exp_f32 v10, v44\n\t"
    "v_mfma_f32_16x16x32_bf16 v[152:155], v[176:179], v[104:107], v[168:171]\n\t"
    "v_exp_f32 v11, v45\n\t"
    "v_mfma_f32_16x16x32_bf16 v[68:71], a[128:131], v[208:211], v[132:135]\n\t"
    "ds_read_b64_tr_b16 a[172:173], v2 offset:256\n\t"
    "v_cvt_pk_bf16_f32 v108, v10, v11\n\t"
    "v_mfma_f32_16x16x32_bf16 v[68:71], a[132:135], v[212:215], v[68:71]\n\t"
    "buffer_load_dwordx4 v188, s[40:43], s58 offen offset:2048 nt lds\n\t"
    "v_mfma_f32_16x16x32_bf16 v[68:71], a[136:139], v[216:219], v[68:71]\n\t"
    "v_exp_f32 v12, v46\n\t"
    "v_mfma_f32_16x16x32_bf16 v[68:71], a[140:143], v[220:223], v[68:71]\n\t"
    "v_exp_f32 v13, v47\n\t"
    "v_mfma_f32_16x16x32_bf16 v[84:87], a[144:147], v[208:211], v[132:135]\n\t"
    "ds_read_b64_tr_b16 a[174:175], v2 offset:4352\n\t"
    "v_cvt_pk_bf16_f32 v109, v12, v13\n\t"
    "v_mfma_f32_16x16x32_bf16 v[84:87], a[148:151], v[212:215], v[84:87]\n\t"
    "ds_read_b64_tr_b16 a[176:177], v1 offset:512\n\t"
    "v_max3_f32 v9, v144, v148, v152\n\t"
    "v_mfma_f32_16x16x32_bf16 v[84:87], a[152:155], v[216:219], v[84:87]\n\t"
    "v_exp_f32 v14, v60\n\t"
    "v_mfma_f32_16x16x32_bf16 v[84:87], a[156:159], v[220:223], v[84:87]\n\t"
    "v_exp_f32 v15, v61\n\t"
    "s_sleep 2\n\t"
    "v_mfma_f32_16x16x32_bf16 v[72:75], a[128:131], v[224:227], v[136:139]\n\t"
    "ds_read_b64_tr_b16 a[178:179], v1 offset:4608\n\t"
    "v_cvt_pk_bf16_f32 v110, v14, v15\n\t"
    "v_mfma_f32_16x16x32_bf16 v[72:75], a[132:135], v[228:231], v[72:75]\n\t"
    "v_exp_f32 v16, v62\n\t"
    "v_mfma_f32_16x16x32_bf16 v[72:75], a[136:139], v[232:235], v[72:75]\n\t"
    "v_exp_f32 v17, v63\n\t"
    "v_mfma_f32_16x16x32_bf16 v[72:75], a[140:143], v[236:239], v[72:75]\n\t"
    "ds_read_b64_tr_b16 a[180:181], v2 offset:512\n\t"
    "v_cvt_pk_bf16_f32 v111, v16, v17\n\t"
    "v_mfma_f32_16x16x32_bf16 v[88:91], a[144:147], v[224:227], v[136:139]\n\t"
    "ds_read_b64_tr_b16 a[182:183], v2 offset:4608\n\t"
    "s_cmp_eq_u32 s61, 81920\n\t"
    "v_mfma_f32_16x16x32_bf16 v[88:91], a[148:151], v[228:231], v[88:91]\n\t"
    "ds_read_b64_tr_b16 a[184:185], v1 offset:768\n\t"
    "s_cselect_b32 s61, 0, s61\n\t"
    "v_mfma_f32_16x16x32_bf16 v[156:159], v[176:179], v[108:111], v[172:175]\n\t"
    "ds_read_b64_tr_b16 a[186:187], v1 offset:4864\n\t"
    "v_add_u32 v0, s61, v3\n\t"
    "v_mfma_f32_16x16x32_bf16 v[88:91], a[152:155], v[232:235], v[88:91]\n\t"
    "ds_read_b64_tr_b16 a[188:189], v2 offset:768\n\t"
    "v_mfma_f32_16x16x32_bf16 v[88:91], a[156:159], v[236:239], v[88:91]\n\t"
    "ds_read_b64_tr_b16 a[190:191], v2 offset:4864\n\t"
    "v_mfma_f32_16x16x32_bf16 v[76:79], a[128:131], v[240:243], v[140:143]\n\t"
    "buffer_load_dwordx4 v189, s[40:43], s58 offen offset:3072 nt lds\n\t"
    "v_mfma_f32_16x16x32_bf16 v[76:79], a[132:135], v[244:247], v[76:79]\n\t"
    "s_add_u32 s58, s58, 16384\n\t"
    "s_memrealtime s[38:39]\n\t"
    "v_mfma_f32_16x16x32_bf16 v[76:79], a[136:139], v[248:251], v[76:79]\n\t"
    "v_max_f32 v9, v9, v156\n\t"
    "v_cmp_le_f32 vcc, s63, v9\n\t"
    "v_mfma_f32_16x16x32_bf16 v[76:79], a[140:143], v[252:255], v[76:79]\n\t"
    "v_mfma_f32_16x16x32_bf16 v[92:95], a[144:147], v[240:243], v[140:143]\n\t"
    "s_cbranch_vccz L_ret0p0_%=\n\t"
    "s_swappc_b64 s[34:35], s[30:31]\n\t"
    "L_ret0p0_%=:\n\t"
    "v_mfma_f32_16x16x32_bf16 v[92:95], a[148:151], v[244:247], v[92:95]\n\t"
    "s_waitcnt lgkmcnt(0)\n\t"
    "v_mfma_f32_16x16x32_bf16 v[92:95], a[152:155], v[248:251], v[92:95]\n\t"
    "s_sub_u32 s37, s36, s38\n\t"
    "s_cmp_gt_i32 s37, 0\n\t"
    "v_mfma_f32_16x16x32_bf16 v[92:95], a[156:159], v[252:255], v[92:95]\n\t"
    "s_cbranch_scc1 L_wait0p0_%=\n\t"
    "L_back0p0_%=:\n\t"
    "s_add_u32 s36, s38, 130\n\t"
    "s_sleep 2\n\t"
    "v_mfma_f32_16x16x32_bf16 a[0:3], a[160:163], v[96:99], a[0:3]\n\t"
    "v_exp_f32 v10, v64\n\t"
    "v_mfma_f32_16x16x32_bf16 a[16:19], a[164:167], v[96:99], a[16:19]\n\t"
    "v_exp_f32 v11, v65\n\t"
    "v_mfma_f32_16x16x32_bf16 a[32:35], a[168:171], v[96:99], a[32:35]\n\t"
    "ds_read_b128 a[128:131], v0 offset:0\n\t"
    "v_cvt_pk_bf16_f32 v112, v10, v11\n\t"
    "v_mfma_f32_16x16x32_bf16 a[48:51], a[172:175], v[96:99], a[48:51]\n\t"
    "v_exp_f32 v12, v66\n\t"
    "v_mfma_f32_16x16x32_bf16 a[64:67], a[176:179], v[96:99], a[64:67]\n\t"
    "v_exp_f32 v13, v67\n\t"
    "v_mfma_f32_16x16x32_bf16 a[80:83], a[180:183], v[96:99], a[80:83]\n\t"
    "ds_read_b128 a[132:135], v0 offset:256\n\t"
    "v_cvt_pk_bf16_f32 v113, v12, v13\n\t"
    "v_mfma_f32_16x16x32_bf16 a[96:99], a[184:187], v[96:99], a[96:99]\n\t"
    "s_add_u32 m0, s59, s57\n\t"
    "s_mov_b32 s59, s60\n\t"
    "v_mfma_f32_16x16x32_bf16 a[112:115], a[188:191], v[96:99], a[112:115]\n\t"
    "v_exp_f32 v14, v80\n\t"
    "v_mfma_f32_16x16x32_bf16 a[4:7], a[160:163], v[100:103], a[4:7]\n\t"
    "v_exp_f32 v15, v81\n\t"
    "v_mfma_f32_16x16x32_bf16 a[20:23], a[164:167], v[100:103], a[20:23]\n\t"
    "ds_read_b128 a[136:139], v0 offset:512\n\t"
    "v_cvt_pk_bf16_f32 v114, v14, v15\n\t"
    "v_mfma_f32_16x16x32_bf16 a[36:39], a[168:171], v[100:103], a[36:39]\n\t"
    "buffer_load_dwordx4 v8, s[44:47], s58 offen nt lds\n\t"
    "v_mfma_f32_16x16x32_bf16 a[52:55], a[172:175], v[100:103], a[52:55]\n\t"
    "v_exp_f32 v16, v82\n\t"
    "v_mfma_f32_16x16x32_bf16 a[68:71], a[176:179], v[100:103], a[68:71]\n\t"
    "v_exp_f32 v17, v83\n\t"
    "v_mfma_f32_16x16x32_bf16 a[84:87], a[180:183], v[100:103], a[84:87]\n\t"
    "ds_read_b128 a[140:143], v0 offset:768\n\t"
    "v_cvt_pk_bf16_f32 v115, v16, v17\n\t"
    "v_mfma_f32_16x16x32_bf16 a[100:103], a[184:187], v[100:103], a[100:103]\n\t"
    "v_exp_f32 v10, v68\n\t"
    "v_mfma_f32_16x16x32_bf16 a[116:119], a[188:191], v[100:103], a[116:119]\n\t"
    "v_exp_f32 v11, v69\n\t"
    "v_mfma_f32_16x16x32_bf16 v[160:163], v[176:179], v[112:115], v[144:147]\n\t"
    "ds_read_b128 a[144:147], v0 offset:4096\n\t"
    "v_cvt_pk_bf16_f32 v116, v10, v11\n\t"
    "s_sleep 2\n\t"
    "v_mfma_f32_16x16x32_bf16 a[8:11], a[160:163], v[104:107], a[8:11]\n\t"
    "v_exp_f32 v12, v70\n\t"
    "v_mfma_f32_16x16x32_bf16 a[24:27], a[164:167], v[104:107], a[24:27]\n\t"
    "v_exp_f32 v13, v71\n\t"
    "v_mfma_f32_16x16x32_bf16 a[40:43], a[168:171], v[104:107], a[40:43]\n\t"
    "ds_read_b128 a[148:151], v0 offset:4352\n\t"
    "v_cvt_pk_bf16_f32 v117, v12, v13\n\t"
    "v_mfma_f32_16x16x32_bf16 a[56:59], a[172:175], v[104:107], a[56:59]\n\t"
    "v_exp_f32 v14, v84\n\t"
    "v_mfma_f32_16x16x32_bf16 a[72:75], a[176:179], v[104:107], a[72:75]\n\t"
    "v_exp_f32 v15, v85\n\t"
    "v_mfma_f32_16x16x32_bf16 a[88:91], a[180:183], v[104:107], a[88:91]\n\t"
    "ds_read_b128 a[152:155], v0 offset:4608\n\t"
    "v_cvt_pk_bf16_f32 v118, v14, v15\n\t"
    "v_mfma_f32_16x16x32_bf16 a[104:107], a[184:187], v[104:107], a[104:107]\n\t"
    "v_exp_f32 v16, v86\n\t"
    "v_mfma_f32_16x16x32_bf16 a[120:123], a[188:191], v[104:107], a[120:123]\n\t"
    "v_exp_f32 v17, v87\n\t"
    "v_mfma_f32_16x16x32_bf16 a[12:15], a[160:163], v[108:111], a[12:15]\n\t"
    "ds_read_b128 a[156:159], v0 offset:4864\n\t"
    "v_cvt_pk_bf16_f32 v119, v16, v17\n\t"
    "v_mfma_f32_16x16x32_bf16 a[28:31], a[164:167], v[108:111], a[28:31]\n\t"
    "buffer_load_dwordx4 v190, s[44:47], s58 offen offset:1024 nt lds\n\t"
    "v_mfma_f32_16x16x32_bf16 a[44:47], a[168:171], v[108:111], a[44:47]\n\t"
    "v_exp_f32 v10, v72\n\t"
    "v_mfma_f32_16x16x32_bf16 v[164:167], v[176:179], v[116:119], v[148:151]\n\t"
    "v_exp_f32 v11, v73\n\t"
    "v_mfma_f32_16x16x32_bf16 a[60:63], a[172:175], v[108:111], a[60:63]\n\t"
    "ds_read_b64_tr_b16 a[160:161], v1 offset:8192\n\t"
    "v_cvt_pk_bf16_f32 v120, v10, v11\n\t"
    "v_mfma_f32_16x16x32_bf16 a[76:79], a[176:179], v[108:111], a[76:79]\n\t"
    "v_exp_f32 v12, v74\n\t"
    "v_mfma_f32_16x16x32_bf16 a[92:95], a[180:183], v[108:111], a[92:95]\n\t"
    "v_exp_f32 v13, v75\n\t"
    "v_mfma_f32_16x16x32_bf16 a[108:111], a[184:187], v[108:111], a[108:111]\n\t"
    "ds_read_b64_tr_b16 a[162:163], v1 offset:12288\n\t"
    "v_cvt_pk_bf16_f32 v121, v12, v13\n\t"
    "v_mfma_f32_16x16x32_bf16 a[124:127], a[188:191], v[108:111], a[124:127]\n\t"
    "ds_read_b64_tr_b16 a[164:165], v2 offset:8192\n\t"
    "s_waitcnt lgkmcnt(3)\n\t"
    "s_sleep 2\n\t"
    "v_mfma_f32_16x16x32_bf16 v[32:35], a[128:131], v[192:195], v[128:131]\n\t"
    "v_exp_f32 v14, v88\n\t"
    "v_mfma_f32_16x16x32_bf16 v[32:35], a[132:135], v[196:199], v[32:35]\n\t"
    "v_exp_f32 v15, v89\n\t"
    "v_mfma_f32_16x16x32_bf16 v[32:35], a[136:139], v[200:203], v[32:35]\n\t"
    "ds_read_b64_tr_b16 a[166:167], v2 offset:12288\n\t"
    "v_cvt_pk_bf16_f32 v122, v14, v15\n\t"
    "v_mfma_f32_16x16x32_bf16 v[32:35], a[140:143], v[204:207], v[32:35]\n\t"
    "v_exp_f32 v16, v90\n\t"
    "v_mfma_f32_16x16x32_bf16 v[48:51], a[144:147], v[192:195], v[128:131]\n\t"
    "v_exp_f32 v17, v91\n\t"
    "v_mfma_f32_16x16x32_bf16 v[48:51], a[148:151], v[196:199], v[48:51]\n\t"
    "ds_read_b64_tr_b16 a[168:169], v1 offset:8448\n\t"
    "v_cvt_pk_bf16_f32 v123, v16, v17\n\t"
    "v_mfma_f32_16x16x32_bf16 v[48:51], a[152:155], v[200:203], v[48:51]\n\t"
    "ds_read_b64_tr_b16 a[170:171], v1 offset:12544\n\t"
    "v_mfma_f32_16x16x32_bf16 v[48:51], a[156:159], v[204:207], v[48:51]\n\t"
    "v_exp_f32 v10, v76\n\t"
    "v_mfma_f32_16x16x32_bf16 v[168:171], v[176:179], v[120:123], v[152:155]\n\t"
    "v_exp_f32 v11, v77\n\t"
    "v_mfma_f32_16x16x32_bf16 v[36:39], a[128:131], v[208:211], v[132:135]\n\t"
    "ds_read_b64_tr_b16 a[172:173], v2 offset:8448\n\t"
    "v_cvt_pk_bf16_f32 v124, v10, v11\n\t"
    "v_mfma_f32_16x16x32_bf16 v[36:39], a[132:135], v[212:215], v[36:39]\n\t"
    "buffer_load_dwordx4 v8, s[44:47], s58 offen offset:2048 nt lds\n\t"
    "v_mfma_f32_16x16x32_bf16 v[36:39], a[136:139], v[216:219], v[36:39]\n\t"
    "v_exp_f32 v12, v78\n\t"
    "v_mfma_f32_16x16x32_bf16 v[36:39], a[140:143], v[220:223], v[36:39]\n\t"
    "v_exp_f32 v13, v79\n\t"
    "v_mfma_f32_16x16x32_bf16 v[52:55], a[144:147], v[208:211], v[132:135]\n\t"
    "ds_read_b64_tr_b16 a[174:175], v2 offset:12544\n\t"
    "v_cvt_pk_bf16_f32 v125, v12, v13\n\t"
    "v_mfma_f32_16x16x32_bf16 v[52:55], a[148:151], v[212:215], v[52:55]\n\t"
    "ds_read_b64_tr_b16 a[176:177], v1 offset:8704\n\t"
    "v_max3_f32 v9, v160, v164, v168\n\t"
    "v_mfma_f32_16x16x32_bf16 v[52:55], a[152:155], v[216:219], v[52:55]\n\t"
    "v_exp_f32 v14, v92\n\t"
    "v_mfma_f32_16x16x32_bf16 v[52:55], a[156:159], v[220:223], v[52:55]\n\t"
    "v_exp_f32 v15, v93\n\t"
    "s_sleep 1\n\t"
    "v_mfma_f32_16x16x32_bf16 v[40:43], a[128:131], v[224:227], v[136:139]\n\t"
    "ds_read_b64_tr_b16 a[178:179], v1 offset:12800\n\t"
    "v_cvt_pk_bf16_f32 v126, v14, v15\n\t"
    "v_mfma_f32_16x16x32_bf16 v[40:43], a[132:135], v[228:231], v[40:43]\n\t"
    "v_exp_f32 v16, v94\n\t"
    "v_mfma_f32_16x16x32_bf16 v[40:43], a[136:139], v[232:235], v[40:43]\n\t"
    "v_exp_f32 v17, v95\n\t"
    "v_mfma_f32_16x16x32_bf16 v[40:43], a[140:143], v[236:239], v[40:43]\n\t"
    "ds_read_b64_tr_b16 a[180:181], v2 offset:8704\n\t"
    "v_cvt_pk_bf16_f32 v127, v16, v17\n\t"
    "v_mfma_f32_16x16x32_bf16 v[56:59], a[144:147], v[224:227], v[136:139]\n\t"
    "ds_read_b64_tr_b16 a[182:183], v2 offset:12800\n\t"
    "s_mov_b32 s60, s61\n\t"
    "v_mfma_f32_16x16x32_bf16 v[56:59], a[148:151], v[228:231], v[56:59]\n\t"
    "ds_read_b64_tr_b16 a[184:185], v1 offset:8960\n\t"
    "v_mfma_f32_16x16x32_bf16 v[172:175], v[176:179], v[124:127], v[156:159]\n\t"
    "ds_read_b64_tr_b16 a[186:187], v1 offset:13056\n\t"
    "v_mfma_f32_16x16x32_bf16 v[56:59], a[152:155], v[232:235], v[56:59]\n\t"
    "ds_read_b64_tr_b16 a[188:189], v2 offset:8960\n\t"
    "v_mfma_f32_16x16x32_bf16 v[56:59], a[156:159], v[236:239], v[56:59]\n\t"
    "ds_read_b64_tr_b16 a[190:191], v2 offset:13056\n\t"
    "v_mfma_f32_16x16x32_bf16 v[44:47], a[128:131], v[240:243], v[140:143]\n\t"
    "buffer_load_dwordx4 v190, s[44:47], s58 offen offset:3072 nt lds\n\t"
    "v_mfma_f32_16x16x32_bf16 v[44:47], a[132:135], v[244:247], v[44:47]\n\t"
    "v_add_u32 v1, s60, v4\n\t"
    "v_add_u32 v2, s60, v5\n\t"
    "v_mfma_f32_16x16x32_bf16 v[44:47], a[136:139], v[248:251], v[44:47]\n\t"
    "v_max_f32 v9, v9, v172\n\t"
    "v_cmp_le_f32 vcc, s63, v9\n\t"
    "v_mfma_f32_16x16x32_bf16 v[44:47], a[140:143], v[252:255], v[44:47]\n\t"
    "v_mfma_f32_16x16x32_bf16 v[60:63], a[144:147], v[240:243], v[140:143]\n\t"
    "s_cbranch_vccz L_ret1p1_%=\n\t"
    "s_swappc_b64 s[34:35], s[32:33]\n\t"
    "L_ret1p1_%=:\n\t"
    "v_mfma_f32_16x16x32_bf16 v[60:63], a[148:151], v[244:247], v[60:63]\n\t"
    "s_waitcnt lgkmcnt(0)\n\t"
    "v_mfma_f32_16x16x32_bf16 v[60:63], a[152:155], v[248:251], v[60:63]\n\t"
    "s_waitcnt vmcnt(20)\n\t"
    "s_barrier\n\t"
    "v_mfma_f32_16x16x32_bf16 v[60:63], a[156:159], v[252:255], v[60:63]\n\t"
    "s_sleep 1\n\t"
    "s_sub_u32 s62, s62, 1\n\t"
    "v_mfma_f32_16x16x32_bf16 a[0:3], a[160:163], v[112:115], a[0:3]\n\t"
    "v_exp_f32 v10, v32\n\t"
    "v_mfma_f32_16x16x32_bf16 a[16:19], a[164:167], v[112:115], a[16:19]\n\t"
    "v_exp_f32 v11, v33\n\t"
    "v_mfma_f32_16x16x32_bf16 a[32:35], a[168:171], v[112:115], a[32:35]\n\t"
    "ds_read_b128 a[128:131], v0 offset:8192\n\t"
    "v_cvt_pk_bf16_f32 v96, v10, v11\n\t"
    "v_mfma_f32_16x16x32_bf16 a[48:51], a[172:175], v[112:115], a[48:51]\n\t"
    "v_exp_f32 v12, v34\n\t"
    "v_mfma_f32_16x16x32_bf16 a[64:67], a[176:179], v[112:115], a[64:67]\n\t"
    "v_exp_f32 v13, v35\n\t"
    "v_mfma_f32_16x16x32_bf16 a[80:83], a[180:183], v[112:115], a[80:83]\n\t"
    "ds_read_b128 a[132:135], v0 offset:8448\n\t"
    "v_cvt_pk_bf16_f32 v97, v12, v13\n\t"
    "v_mfma_f32_16x16x32_bf16 a[96:99], a[184:187], v[112:115], a[96:99]\n\t"
    "s_add_u32 m0, s59, s56\n\t"
    "s_add_u32 s61, s61, 16384\n\t"
    "v_mfma_f32_16x16x32_bf16 a[112:115], a[188:191], v[112:115], a[112:115]\n\t"
    "v_exp_f32 v14, v48\n\t"
    "v_mfma_f32_16x16x32_bf16 a[4:7], a[160:163], v[116:119], a[4:7]\n\t"
    "v_exp_f32 v15, v49\n\t"
    "v_mfma_f32_16x16x32_bf16 a[20:23], a[164:167], v[116:119], a[20:23]\n\t"
    "ds_read_b128 a[136:139], v0 offset:8704\n\t"
    "v_cvt_pk_bf16_f32 v98, v14, v15\n\t"
    "v_mfma_f32_16x16x32_bf16 a[36:39], a[168:171], v[116:119], a[36:39]\n\t"
    "buffer_load_dwordx4 v6, s[40:43], s58 offen nt lds\n\t"
    "v_mfma_f32_16x16x32_bf16 a[52:55], a[172:175], v[116:119], a[52:55]\n\t"
    "v_exp_f32 v16, v50\n\t"
    "v_mfma_f32_16x16x32_bf16 a[68:71], a[176:179], v[116:119], a[68:71]\n\t"
    "v_exp_f32 v17, v51\n\t"
    "v_mfma_f32_16x16x32_bf16 a[84:87], a[180:183], v[116:119], a[84:87]\n\t"
    "ds_read_b128 a[140:143], v0 offset:8960\n\t"
    "v_cvt_pk_bf16_f32 v99, v16, v17\n\t"
    "v_mfma_f32_16x16x32_bf16 a[100:103], a[184:187], v[116:119], a[100:103]\n\t"
    "v_exp_f32 v10, v36\n\t"
    "v_mfma_f32_16x16x32_bf16 a[116:119], a[188:191], v[116:119], a[116:119]\n\t"
    "v_exp_f32 v11, v37\n\t"
    "v_mfma_f32_16x16x32_bf16 v[144:147], v[176:179], v[96:99], v[160:163]\n\t"
    "ds_read_b128 a[144:147], v0 offset:12288\n\t"
    "v_cvt_pk_bf16_f32 v100, v10, v11\n\t"
    "s_sleep 1\n\t"
    "v_mfma_f32_16x16x32_bf16 a[8:11], a[160:163], v[120:123], a[8:11]\n\t"
    "v_exp_f32 v12, v38\n\t"
    "v_mfma_f32_16x16x32_bf16 a[24:27], a[164:167], v[120:123], a[24:27]\n\t"
    "v_exp_f32 v13, v39\n\t"
    "v_mfma_f32_16x16x32_bf16 a[40:43], a[168:171], v[120:123], a[40:43]\n\t"
    "ds_read_b128 a[148:151], v0 offset:12544\n\t"
    "v_cvt_pk_bf16_f32 v101, v12, v13\n\t"
    "v_mfma_f32_16x16x32_bf16 a[56:59], a[172:175], v[120:123], a[56:59]\n\t"
    "v_exp_f32 v14, v52\n\t"
    "v_mfma_f32_16x16x32_bf16 a[72:75], a[176:179], v[120:123], a[72:75]\n\t"
    "v_exp_f32 v15, v53\n\t"
    "v_mfma_f32_16x16x32_bf16 a[88:91], a[180:183], v[120:123], a[88:91]\n\t"
    "ds_read_b128 a[152:155], v0 offset:12800\n\t"
    "v_cvt_pk_bf16_f32 v102, v14, v15\n\t"
    "v_mfma_f32_16x16x32_bf16 a[104:107], a[184:187], v[120:123], a[104:107]\n\t"
    "v_exp_f32 v16, v54\n\t"
    "v_mfma_f32_16x16x32_bf16 a[120:123], a[188:191], v[120:123], a[120:123]\n\t"
    "v_exp_f32 v17, v55\n\t"
    "v_mfma_f32_16x16x32_bf16 a[12:15], a[160:163], v[124:127], a[12:15]\n\t"
    "ds_read_b128 a[156:159], v0 offset:13056\n\t"
    "v_cvt_pk_bf16_f32 v103, v16, v17\n\t"
    "v_mfma_f32_16x16x32_bf16 a[28:31], a[164:167], v[124:127], a[28:31]\n\t"
    "buffer_load_dwordx4 v7, s[40:43], s58 offen offset:1024 nt lds\n\t"
    "v_mfma_f32_16x16x32_bf16 a[44:47], a[168:171], v[124:127], a[44:47]\n\t"
    "v_exp_f32 v10, v40\n\t"
    "v_mfma_f32_16x16x32_bf16 v[148:151], v[176:179], v[100:103], v[164:167]\n\t"
    "v_exp_f32 v11, v41\n\t"
    "v_mfma_f32_16x16x32_bf16 a[60:63], a[172:175], v[124:127], a[60:63]\n\t"
    "ds_read_b64_tr_b16 a[160:161], v1 offset:0\n\t"
    "v_cvt_pk_bf16_f32 v104, v10, v11\n\t"
    "v_mfma_f32_16x16x32_bf16 a[76:79], a[176:179], v[124:127], a[76:79]\n\t"
    "v_exp_f32 v12, v42\n\t"
    "v_mfma_f32_16x16x32_bf16 a[92:95], a[180:183], v[124:127], a[92:95]\n\t"
    "v_exp_f32 v13, v43\n\t"
    "v_mfma_f32_16x16x32_bf16 a[108:111], a[184:187], v[124:127], a[108:111]\n\t"
    "ds_read_b64_tr_b16 a[162:163], v1 offset:4096\n\t"
    "v_cvt_pk_bf16_f32 v105, v12, v13\n\t"
    "v_mfma_f32_16x16x32_bf16 a[124:127], a[188:191], v[124:127], a[124:127]\n\t"
    "ds_read_b64_tr_b16 a[164:165], v2 offset:0\n\t"
    "s_waitcnt lgkmcnt(3)\n\t"
    "s_sleep 1\n\t"
    "v_mfma_f32_16x16x32_bf16 v[64:67], a[128:131], v[192:195], v[128:131]\n\t"
    "v_exp_f32 v14, v56\n\t"
    "v_mfma_f32_16x16x32_bf16 v[64:67], a[132:135], v[196:199], v[64:67]\n\t"
    "v_exp_f32 v15, v57\n\t"
    "v_mfma_f32_16x16x32_bf16 v[64:67], a[136:139], v[200:203], v[64:67]\n\t"
    "ds_read_b64_tr_b16 a[166:167], v2 offset:4096\n\t"
    "v_cvt_pk_bf16_f32 v106, v14, v15\n\t"
    "v_mfma_f32_16x16x32_bf16 v[64:67], a[140:143], v[204:207], v[64:67]\n\t"
    "v_exp_f32 v16, v58\n\t"
    "v_mfma_f32_16x16x32_bf16 v[80:83], a[144:147], v[192:195], v[128:131]\n\t"
    "v_exp_f32 v17, v59\n\t"
    "v_mfma_f32_16x16x32_bf16 v[80:83], a[148:151], v[196:199], v[80:83]\n\t"
    "ds_read_b64_tr_b16 a[168:169], v1 offset:256\n\t"
    "v_cvt_pk_bf16_f32 v107, v16, v17\n\t"
    "v_mfma_f32_16x16x32_bf16 v[80:83], a[152:155], v[200:203], v[80:83]\n\t"
    "ds_read_b64_tr_b16 a[170:171], v1 offset:4352\n\t"
    "v_mfma_f32_16x16x32_bf16 v[80:83], a[156:159], v[204:207], v[80:83]\n\t"
    "v_exp_f32 v10, v44\n\t"
    "v_mfma_f32_16x16x32_bf16 v[152:155], v[176:179], v[104:107], v[168:171]\n\t"
    "v_exp_f32 v11, v45\n\t"
    "v_mfma_f32_16x16x32_bf16 v[68:71], a[128:131], v[208:211], v[132:135]\n\t"
    "ds_read_b64_tr_b16 a[172:173], v2 offset:256\n\t"
    "v_cvt_pk_bf16_f32 v108, v10, v11\n\t"
    "v_mfma_f32_16x16x32_bf16 v[68:71], a[132:135], v[212:215], v[68:71]\n\t"
    "buffer_load_dwordx4 v188, s[40:43], s58 offen offset:2048 nt lds\n\t"
    "v_mfma_f32_16x16x32_bf16 v[68:71], a[136:139], v[216:219], v[68:71]\n\t"
    "v_exp_f32 v12, v46\n\t"
    "v_mfma_f32_16x16x32_bf16 v[68:71], a[140:143], v[220:223], v[68:71]\n\t"
    "v_exp_f32 v13, v47\n\t"
    "v_mfma_f32_16x16x32_bf16 v[84:87], a[144:147], v[208:211], v[132:135]\n\t"
    "ds_read_b64_tr_b16 a[174:175], v2 offset:4352\n\t"
    "v_cvt_pk_bf16_f32 v109, v12, v13\n\t"
    "v_mfma_f32_16x16x32_bf16 v[84:87], a[148:151], v[212:215], v[84:87]\n\t"
    "ds_read_b64_tr_b16 a[176:177], v1 offset:512\n\t"
    "v_max3_f32 v9, v144, v148, v152\n\t"
    "v_mfma_f32_16x16x32_bf16 v[84:87], a[152:155], v[216:219], v[84:87]\n\t"
    "v_exp_f32 v14, v60\n\t"
    "v_mfma_f32_16x16x32_bf16 v[84:87], a[156:159], v[220:223], v[84:87]\n\t"
    "v_exp_f32 v15, v61\n\t"
    "s_sleep 1\n\t"
    "v_mfma_f32_16x16x32_bf16 v[72:75], a[128:131], v[224:227], v[136:139]\n\t"
    "ds_read_b64_tr_b16 a[178:179], v1 offset:4608\n\t"
    "v_cvt_pk_bf16_f32 v110, v14, v15\n\t"
    "v_mfma_f32_16x16x32_bf16 v[72:75], a[132:135], v[228:231], v[72:75]\n\t"
    "v_exp_f32 v16, v62\n\t"
    "v_mfma_f32_16x16x32_bf16 v[72:75], a[136:139], v[232:235], v[72:75]\n\t"
    "v_exp_f32 v17, v63\n\t"
    "v_mfma_f32_16x16x32_bf16 v[72:75], a[140:143], v[236:239], v[72:75]\n\t"
    "ds_read_b64_tr_b16 a[180:181], v2 offset:512\n\t"
    "v_cvt_pk_bf16_f32 v111, v16, v17\n\t"
    "v_mfma_f32_16x16x32_bf16 v[88:91], a[144:147], v[224:227], v[136:139]\n\t"
    "ds_read_b64_tr_b16 a[182:183], v2 offset:4608\n\t"
    "s_cmp_eq_u32 s61, 81920\n\t"
    "v_mfma_f32_16x16x32_bf16 v[88:91], a[148:151], v[228:231], v[88:91]\n\t"
    "ds_read_b64_tr_b16 a[184:185], v1 offset:768\n\t"
    "s_cselect_b32 s61, 0, s61\n\t"
    "v_mfma_f32_16x16x32_bf16 v[156:159], v[176:179], v[108:111], v[172:175]\n\t"
    "ds_read_b64_tr_b16 a[186:187], v1 offset:4864\n\t"
    "v_add_u32 v0, s61, v3\n\t"
    "v_mfma_f32_16x16x32_bf16 v[88:91], a[152:155], v[232:235], v[88:91]\n\t"
    "ds_read_b64_tr_b16 a[188:189], v2 offset:768\n\t"
    "v_mfma_f32_16x16x32_bf16 v[88:91], a[156:159], v[236:239], v[88:91]\n\t"
    "ds_read_b64_tr_b16 a[190:191], v2 offset:4864\n\t"
    "v_mfma_f32_16x16x32_bf16 v[76:79], a[128:131], v[240:243], v[140:143]\n\t"
    "buffer_load_dwordx4 v189, s[40:43], s58 offen offset:3072 nt lds\n\t"
    "v_mfma_f32_16x16x32_bf16 v[76:79], a[132:135], v[244:247], v[76:79]\n\t"
    "s_add_u32 s58, s58, 16384\n\t"
    "s_memrealtime s[38:39]\n\t"
    "v_mfma_f32_16x16x32_bf16 v[76:79], a[136:139], v[248:251], v[76:79]\n\t"
    "v_max_f32 v9, v9, v156\n\t"
    "v_cmp_le_f32 vcc, s63, v9\n\t"
    "v_mfma_f32_16x16x32_bf16 v[76:79], a[140:143], v[252:255], v[76:79]\n\t"
    "v_mfma_f32_16x16x32_bf16 v[92:95], a[144:147], v[240:243], v[140:143]\n\t"
    "s_cbranch_vccz L_ret0p2_%=\n\t"
    "s_swappc_b64 s[34:35], s[30:31]\n\t"
    "L_ret0p2_%=:\n\t"
    "v_mfma_f32_16x16x32_bf16 v[92:95], a[148:151], v[244:247], v[92:95]\n\t"
    "s_waitcnt lgkmcnt(0)\n\t"
    "v_mfma_f32_16x16x32_bf16 v[92:95], a[152:155], v[248:251], v[92:95]\n\t"
    "s_sub_u32 s37, s36, s38\n\t"
    "s_cmp_gt_i32 s37, 0\n\t"
    "v_mfma_f32_16x16x32_bf16 v[92:95], a[156:159], v[252:255], v[92:95]\n\t"
    "s_cbranch_scc1 L_wait0p2_%=\n\t"
    "L_back0p2_%=:\n\t"
    "s_add_u32 s36, s38, 130\n\t"
    "s_sleep 1\n\t"
    "v_mfma_f32_16x16x32_bf16 a[0:3], a[160:163], v[96:99], a[0:3]\n\t"
    "v_exp_f32 v10, v64\n\t"
    "v_mfma_f32_16x16x32_bf16 a[16:19], a[164:167], v[96:99], a[16:19]\n\t"
    "v_exp_f32 v11, v65\n\t"
    "v_mfma_f32_16x16x32_bf16 a[32:35], a[168:171], v[96:99], a[32:35]\n\t"
    "ds_read_b128 a[128:131], v0 offset:0\n\t"
    "v_cvt_pk_bf16_f32 v112, v10, v11\n\t"
    "v_mfma_f32_16x16x32_bf16 a[48:51], a[172:175], v[96:99], a[48:51]\n\t"
    "v_exp_f32 v12, v66\n\t"
    "v_mfma_f32_16x16x32_bf16 a[64:67], a[176:179], v[96:99], a[64:67]\n\t"
    "v_exp_f32 v13, v67\n\t"
    "v_mfma_f32_16x16x32_bf16 a[80:83], a[180:183], v[96:99], a[80:83]\n\t"
    "ds_read_b128 a[132:135], v0 offset:256\n\t"
    "v_cvt_pk_bf16_f32 v113, v12, v13\n\t"
    "v_mfma_f32_16x16x32_bf16 a[96:99], a[184:187], v[96:99], a[96:99]\n\t"
    "s_add_u32 m0, s59, s57\n\t"
    "s_mov_b32 s59, s60\n\t"
    "v_mfma_f32_16x16x32_bf16 a[112:115], a[188:191], v[96:99], a[112:115]\n\t"
    "v_exp_f32 v14, v80\n\t"
    "v_mfma_f32_16x16x32_bf16 a[4:7], a[160:163], v[100:103], a[4:7]\n\t"
    "v_exp_f32 v15, v81\n\t"
    "v_mfma_f32_16x16x32_bf16 a[20:23], a[164:167], v[100:103], a[20:23]\n\t"
    "ds_read_b128 a[136:139], v0 offset:512\n\t"
    "v_cvt_pk_bf16_f32 v114, v14, v15\n\t"
    "v_mfma_f32_16x16x32_bf16 a[36:39], a[168:171], v[100:103], a[36:39]\n\t"
    "buffer_load_dwordx4 v8, s[44:47], s58 offen nt lds\n\t"
    "v_mfma_f32_16x16x32_bf16 a[52:55], a[172:175], v[100:103], a[52:55]\n\t"
    "v_exp_f32 v16, v82\n\t"
    "v_mfma_f32_16x16x32_bf16 a[68:71], a[176:179], v[100:103], a[68:71]\n\t"
    "v_exp_f32 v17, v83\n\t"
    "v_mfma_f32_16x16x32_bf16 a[84:87], a[180:183], v[100:103], a[84:87]\n\t"
    "ds_read_b128 a[140:143], v0 offset:768\n\t"
    "v_cvt_pk_bf16_f32 v115, v16, v17\n\t"
    "v_mfma_f32_16x16x32_bf16 a[100:103], a[184:187], v[100:103], a[100:103]\n\t"
    "v_exp_f32 v10, v68\n\t"
    "v_mfma_f32_16x16x32_bf16 a[116:119], a[188:191], v[100:103], a[116:119]\n\t"
    "v_exp_f32 v11, v69\n\t"
    "v_mfma_f32_16x16x32_bf16 v[160:163], v[176:179], v[112:115], v[144:147]\n\t"
    "ds_read_b128 a[144:147], v0 offset:4096\n\t"
    "v_cvt_pk_bf16_f32 v116, v10, v11\n\t"
    "v_mfma_f32_16x16x32_bf16 a[8:11], a[160:163], v[104:107], a[8:11]\n\t"
    "v_exp_f32 v12, v70\n\t"
    "v_mfma_f32_16x16x32_bf16 a[24:27], a[164:167], v[104:107], a[24:27]\n\t"
    "v_exp_f32 v13, v71\n\t"
    "v_mfma_f32_16x16x32_bf16 a[40:43], a[168:171], v[104:107], a[40:43]\n\t"
    "ds_read_b128 a[148:151], v0 offset:4352\n\t"
    "v_cvt_pk_bf16_f32 v117, v12, v13\n\t"
    "v_mfma_f32_16x16x32_bf16 a[56:59], a[172:175], v[104:107], a[56:59]\n\t"
    "v_exp_f32 v14, v84\n\t"
    "v_mfma_f32_16x16x32_bf16 a[72:75], a[176:179], v[104:107], a[72:75]\n\t"
    "v_exp_f32 v15, v85\n\t"
    "s_sleep 1\n\t"
    "v_mfma_f32_16x16x32_bf16 a[88:91], a[180:183], v[104:107], a[88:91]\n\t"
    "ds_read_b128 a[152:155], v0 offset:4608\n\t"
    "v_cvt_pk_bf16_f32 v118, v14, v15\n\t"
    "v_mfma_f32_16x16x32_bf16 a[104:107], a[184:187], v[104:107], a[104:107]\n\t"
    "v_exp_f32 v16, v86\n\t"
    "v_mfma_f32_16x16x32_bf16 a[120:123], a[188:191], v[104:107], a[120:123]\n\t"
    "v_exp_f32 v17, v87\n\t"
    "v_mfma_f32_16x16x32_bf16 a[12:15], a[160:163], v[108:111], a[12:15]\n\t"
    "ds_read_b128 a[156:159], v0 offset:4864\n\t"
    "v_cvt_pk_bf16_f32 v119, v16, v17\n\t"
    "v_mfma_f32_16x16x32_bf16 a[28:31], a[164:167], v[108:111], a[28:31]\n\t"
    "buffer_load_dwordx4 v190, s[44:47], s58 offen offset:1024 nt lds\n\t"
    "v_mfma_f32_16x16x32_bf16 a[44:47], a[168:171], v[108:111], a[44:47]\n\t"
    "v_exp_f32 v10, v72\n\t"
    "v_mfma_f32_16x16x32_bf16 v[164:167], v[176:179], v[116:119], v[148:151]\n\t"
    "v_exp_f32 v11, v73\n\t"
    "v_mfma_f32_16x16x32_bf16 a[60:63], a[172:175], v[108:111], a[60:63]\n\t"
    "ds_read_b64_tr_b16 a[160:161], v1 offset:8192\n\t"
    "v_cvt_pk_bf16_f32 v120, v10, v11\n\t"
    "v_mfma_f32_16x16x32_bf16 a[76:79], a[176:179], v[108:111], a[76:79]\n\t"
    "v_exp_f32 v12, v74\n\t"
    "v_mfma_f32_16x16x32_bf16 a[92:95], a[180:183], v[108:111], a[92:95]\n\t"
    "v_exp_f32 v13, v75\n\t"
    "v_mfma_f32_16x16x32_bf16 a[108:111], a[184:187], v[108:111], a[108:111]\n\t"
    "ds_read_b64_tr_b16 a[162:163], v1 offset:12288\n\t"
    "v_cvt_pk_bf16_f32 v121, v12, v13\n\t"
    "v_mfma_f32_16x16x32_bf16 a[124:127], a[188:191], v[108:111], a[124:127]\n\t"
    "ds_read_b64_tr_b16 a[164:165], v2 offset:8192\n\t"
    "s_waitcnt lgkmcnt(3)\n\t"
    "v_mfma_f32_16x16x32_bf16 v[32:35], a[128:131], v[192:195], v[128:131]\n\t"
    "v_exp_f32 v14, v88\n\t"
    "v_mfma_f32_16x16x32_bf16 v[32:35], a[132:135], v[196:199], v[32:35]\n\t"
    "v_exp_f32 v15, v89\n\t"
    "v_mfma_f32_16x16x32_bf16 v[32:35], a[136:139], v[200:203], v[32:35]\n\t"
    "ds_read_b64_tr_b16 a[166:167], v2 offset:12288\n\t"
    "v_cvt_pk_bf16_f32 v122, v14, v15\n\t"
    "v_mfma_f32_16x16x32_bf16 v[32:35], a[140:143], v[204:207], v[32:35]\n\t"
    "v_exp_f32 v16, v90\n\t"
    "v_mfma_f32_16x16x32_bf16 v[48:51], a[144:147], v[192:195], v[128:131]\n\t"
    "v_exp_f32 v17, v91\n\t"
    "v_mfma_f32_16x16x32_bf16 v[48:51], a[148:151], v[196:199], v[48:51]\n\t"
    "ds_read_b64_tr_b16 a[168:169], v1 offset:8448\n\t"
    "v_cvt_pk_bf16_f32 v123, v16, v17\n\t"
    "v_mfma_f32_16x16x32_bf16 v[48:51], a[152:155], v[200:203], v[48:51]\n\t"
    "ds_read_b64_tr_b16 a[170:171], v1 offset:12544\n\t"
    "v_mfma_f32_16x16x32_bf16 v[48:51], a[156:159], v[204:207], v[48:51]\n\t"
    "v_exp_f32 v10, v76\n\t"
    "v_mfma_f32_16x16x32_bf16 v[168:171], v[176:179], v[120:123], v[152:155]\n\t"
    "v_exp_f32 v11, v77\n\t"
    "v_mfma_f32_16x16x32_bf16 v[36:39], a[128:131], v[208:211], v[132:135]\n\t"
    "ds_read_b64_tr_b16 a[172:173], v2 offset:8448\n\t"
    "v_cvt_pk_bf16_f32 v124, v10, v11\n\t"
    "v_mfma_f32_16x16x32_bf16 v[36:39], a[132:135], v[212:215], v[36:39]\n\t"
    "buffer_load_dwordx4 v8, s[44:47], s58 offen offset:2048 nt lds\n\t"
    "s_sleep 1\n\t"
    "v_mfma_f32_16x16x32_bf16 v[36:39], a[136:139], v[216:219], v[36:39]\n\t"
    "v_exp_f32 v12, v78\n\t"
    "v_mfma_f32_16x16x32_bf16 v[36:39], a[140:143], v[220:223], v[36:39]\n\t"
    "v_exp_f32 v13, v79\n\t"
    "v_mfma_f32_16x16x32_bf16 v[52:55], a[144:147], v[208:211], v[132:135]\n\t"
    "ds_read_b64_tr_b16 a[174:175], v2 offset:12544\n\t"
    "v_cvt_pk_bf16_f32 v125, v12, v13\n\t"
    "v_mfma_f32_16x16x32_bf16 v[52:55], a[148:151], v[212:215], v[52:55]\n\t"
    "ds_read_b64_tr_b16 a[176:177], v1 offset:8704\n\t"
    "v_max3_f32 v9, v160, v164, v168\n\t"
    "v_mfma_f32_16x16x32_bf16 v[52:55], a[152:155], v[216:219], v[52:55]\n\t"
    "v_exp_f32 v14, v92\n\t"
    "v_mfma_f32_16x16x32_bf16 v[52:55], a[156:159], v[220:223], v[52:55]\n\t"
    "v_exp_f32 v15, v93\n\t"
    "v_mfma_f32_16x16x32_bf16 v[40:43], a[128:131], v[224:227], v[136:139]\n\t"
    "ds_read_b64_tr_b16 a[178:179], v1 offset:12800\n\t"
    "v_cvt_pk_bf16_f32 v126, v14, v15\n\t"
    "v_mfma_f32_16x16x32_bf16 v[40:43], a[132:135], v[228:231], v[40:43]\n\t"
    "v_exp_f32 v16, v94\n\t"
    "v_mfma_f32_16x16x32_bf16 v[40:43], a[136:139], v[232:235], v[40:43]\n\t"
    "v_exp_f32 v17, v95\n\t"
    "v_mfma_f32_16x16x32_bf16 v[40:43], a[140:143], v[236:239], v[40:43]\n\t"
    "ds_read_b64_tr_b16 a[180:181], v2 offset:8704\n\t"
    "v_cvt_pk_bf16_f32 v127, v16, v17\n\t"
    "v_mfma_f32_16x16x32_bf16 v[56:59], a[144:147], v[224:227], v[136:139]\n\t"
    "ds_read_b64_tr_b16 a[182:183], v2 offset:12800\n\t"
    "s_mov_b32 s60, s61\n\t"
    "v_mfma_f32_16x16x32_bf16 v[56:59], a[148:151], v[228:231], v[56:59]\n\t"
    "ds_read_b64_tr_b16 a[184:185], v1 offset:8960\n\t"
    "v_mfma_f32_16x16x32_bf16 v[172:175], v[176:179], v[124:127], v[156:159]\n\t"
    "ds_read_b64_tr_b16 a[186:187], v1 offset:13056\n\t"
    "v_mfma_f32_16x16x32_bf16 v[56:59], a[152:155], v[232:235], v[56:59]\n\t"
    "ds_read_b64_tr_b16 a[188:189], v2 offset:8960\n\t"
    "v_mfma_f32_16x16x32_bf16 v[56:59], a[156:159], v[236:239], v[56:59]\n\t"
    "ds_read_b64_tr_b16 a[190:191], v2 offset:13056\n\t"
    "v_mfma_f32_16x16x32_bf16 v[44:47], a[128:131], v[240:243], v[140:143]\n\t"
    "buffer_load_dwordx4 v190, s[44:47], s58 offen offset:3072 nt lds\n\t"
    "v_mfma_f32_16x16x32_bf16 v[44:47], a[132:135], v[244:247], v[44:47]\n\t"
    "v_add_u32 v1, s60, v4\n\t"
    "v_add_u32 v2, s60, v5\n\t"
    "v_mfma_f32_16x16x32_bf16 v[44:47], a[136:139], v[248:251], v[44:47]\n\t"
    "v_max_f32 v9, v9, v172\n\t"
    "v_cmp_le_f32 vcc, s63, v9\n\t"
    "v_mfma_f32_16x16x32_bf16 v[44:47], a[140:143], v[252:255], v[44:47]\n\t"
    "v_mfma_f32_16x16x32_bf16 v[60:63], a[144:147], v[240:243], v[140:143]\n\t"
    "s_cbranch_vccz L_ret1p3_%=\n\t"
    "s_swappc_b64 s[34:35], s[32:33]\n\t"
    "L_ret1p3_%=:\n\t"
    "v_mfma_f32_16x16x32_bf16 v[60:63], a[148:151], v[244:247], v[60:63]\n\t"
    "s_waitcnt lgkmcnt(0)\n\t"
    "v_mfma_f32_16x16x32_bf16 v[60:63], a[152:155], v[248:251], v[60:63]\n\t"
    "s_waitcnt vmcnt(20)\n\t"
    "s_barrier\n\t"
    "v_mfma_f32_16x16x32_bf16 v[60:63], a[156:159], v[252:255], v[60:63]\n\t"
    "s_sleep 1\n\t"
    "s_sub_u32 s62, s62, 1\n\t"
    "v_mfma_f32_16x16x32_bf16 a[0:3], a[160:163], v[112:115], a[0:3]\n\t"
    "v_exp_f32 v10, v32\n\t"
    "v_mfma_f32_16x16x32_bf16 a[16:19], a[164:167], v[112:115], a[16:19]\n\t"
    "v_exp_f32 v11, v33\n\t"
    "v_mfma_f32_16x16x32_bf16 a[32:35], a[168:171], v[112:115], a[32:35]\n\t"
    "ds_read_b128 a[128:131], v0 offset:8192\n\t"
    "v_cvt_pk_bf16_f32 v96, v10, v11\n\t"
    "v_mfma_f32_16x16x32_bf16 a[48:51], a[172:175], v[112:115], a[48:51]\n\t"
    "v_exp_f32 v12, v34\n\t"
    "v_mfma_f32_16x16x32_bf16 a[64:67], a[176:179], v[112:115], a[64:67]\n\t"
    "v_exp_f32 v13, v35\n\t"
    "v_mfma_f32_16x16x32_bf16 a[80:83], a[180:183], v[112:115], a[80:83]\n\t"
    "ds_read_b128 a[132:135], v0 offset:8448\n\t"
    "v_cvt_pk_bf16_f32 v97, v12, v13\n\t"
    "v_mfma_f32_16x16x32_bf16 a[96:99], a[184:187], v[112:115], a[96:99]\n\t"
    "s_add_u32 m0, s59, s56\n\t"
    "s_add_u32 s61, s61, 16384\n\t"
    "v_mfma_f32_16x16x32_bf16 a[112:115], a[188:191], v[112:115], a[112:115]\n\t"
    "v_exp_f32 v14, v48\n\t"
    "v_mfma_f32_16x16x32_bf16 a[4:7], a[160:163], v[116:119], a[4:7]\n\t"
    "v_exp_f32 v15, v49\n\t"
    "v_mfma_f32_16x16x32_bf16 a[20:23], a[164:167], v[116:119], a[20:23]\n\t"
    "ds_read_b128 a[136:139], v0 offset:8704\n\t"
    "v_cvt_pk_bf16_f32 v98, v14, v15\n\t"
    "v_mfma_f32_16x16x32_bf16 a[36:39], a[168:171], v[116:119], a[36:39]\n\t"
    "buffer_load_dwordx4 v6, s[40:43], s58 offen nt lds\n\t"
    "v_mfma_f32_16x16x32_bf16 a[52:55], a[172:175], v[116:119], a[52:55]\n\t"
    "v_exp_f32 v16, v50\n\t"
    "v_mfma_f32_16x16x32_bf16 a[68:71], a[176:179], v[116:119], a[68:71]\n\t"
    "v_exp_f32 v17, v51\n\t"
    "v_mfma_f32_16x16x32_bf16 a[84:87], a[180:183], v[116:119], a[84:87]\n\t"
    "ds_read_b128 a[140:143], v0 offset:8960\n\t"
    "v_cvt_pk_bf16_f32 v99, v16, v17\n\t"
    "v_mfma_f32_16x16x32_bf16 a[100:103], a[184:187], v[116:119], a[100:103]\n\t"
    "v_exp_f32 v10, v36\n\t"
    "v_mfma_f32_16x16x32_bf16 a[116:119], a[188:191], v[116:119], a[116:119]\n\t"
    "v_exp_f32 v11, v37\n\t"
    "v_mfma_f32_16x16x32_bf16 v[144:147], v[176:179], v[96:99], v[160:163]\n\t"
    "ds_read_b128 a[144:147], v0 offset:12288\n\t"
    "v_cvt_pk_bf16_f32 v100, v10, v11\n\t"
    "v_mfma_f32_16x16x32_bf16 a[8:11], a[160:163], v[120:123], a[8:11]\n\t"
    "v_exp_f32 v12, v38\n\t"
    "v_mfma_f32_16x16x32_bf16 a[24:27], a[164:167], v[120:123], a[24:27]\n\t"
    "v_exp_f32 v13, v39\n\t"
    "v_mfma_f32_16x16x32_bf16 a[40:43], a[168:171], v[120:123], a[40:43]\n\t"
    "ds_read_b128 a[148:151], v0 offset:12544\n\t"
    "v_cvt_pk_bf16_f32 v101, v12, v13\n\t"
    "v_mfma_f32_16x16x32_bf16 a[56:59], a[172:175], v[120:123], a[56:59]\n\t"
    "v_exp_f32 v14, v52\n\t"
    "v_mfma_f32_16x16x32_bf16 a[72:75], a[176:179], v[120:123], a[72:75]\n\t"
    "v_exp_f32 v15, v53\n\t"
    "v_mfma_f32_16x16x32_bf16 a[88:91], a[180:183], v[120:123], a[88:91]\n\t"
    "ds_read_b128 a[152:155], v0 offset:12800\n\t"
    "v_cvt_pk_bf16_f32 v102, v14, v15\n\t"
    "v_mfma_f32_16x16x32_bf16 a[104:107], a[184:187], v[120:123], a[104:107]\n\t"
    "v_exp_f32 v16, v54\n\t"
    "v_mfma_f32_16x16x32_bf16 a[120:123], a[188:191], v[120:123], a[120:123]\n\t"
    "v_exp_f32 v17, v55\n\t"
    "v_mfma_f32_16x16x32_bf16 a[12:15], a[160:163], v[124:127], a[12:15]\n\t"
    "ds_read_b128 a[156:159], v0 offset:13056\n\t"
    "v_cvt_pk_bf16_f32 v103, v16, v17\n\t"
    "v_mfma_f32_16x16x32_bf16 a[28:31], a[164:167], v[124:127], a[28:31]\n\t"
    "buffer_load_dwordx4 v7, s[40:43], s58 offen offset:1024 nt lds\n\t"
    "v_mfma_f32_16x16x32_bf16 a[44:47], a[168:171], v[124:127], a[44:47]\n\t"
    "v_exp_f32 v10, v40\n\t"
    "v_mfma_f32_16x16x32_bf16 v[148:151], v[176:179], v[100:103], v[164:167]\n\t"
    "v_exp_f32 v11, v41\n\t"
    "v_mfma_f32_16x16x32_bf16 a[60:63], a[172:175], v[124:127], a[60:63]\n\t"
    "ds_read_b64_tr_b16 a[160:161], v1 offset:0\n\t"
    "v_cvt_pk_bf16_f32 v104, v10, v11\n\t"
    "v_mfma_f32_16x16x32_bf16 a[76:79], a[176:179], v[124:127], a[76:79]\n\t"
    "v_exp_f32 v12, v42\n\t"
    "v_mfma_f32_16x16x32_bf16 a[92:95], a[180:183], v[124:127], a[92:95]\n\t"
    "v_exp_f32 v13, v43\n\t"
    "v_mfma_f32_16x16x32_bf16 a[108:111], a[184:187], v[124:127], a[108:111]\n\t"
    "ds_read_b64_tr_b16 a[162:163], v1 offset:4096\n\t"
    "v_cvt_pk_bf16_f32 v105, v12, v13\n\t"
    "v_mfma_f32_16x16x32_bf16 a[124:127], a[188:191], v[124:127], a[124:127]\n\t"
    "ds_read_b64_tr_b16 a[164:165], v2 offset:0\n\t"
    "s_waitcnt lgkmcnt(3)\n\t"
    "s_sleep 1\n\t"
    "v_mfma_f32_16x16x32_bf16 v[64:67], a[128:131], v[192:195], v[128:131]\n\t"
    "v_exp_f32 v14, v56\n\t"
    "v_mfma_f32_16x16x32_bf16 v[64:67], a[132:135], v[196:199], v[64:67]\n\t"
    "v_exp_f32 v15, v57\n\t"
    "v_mfma_f32_16x16x32_bf16 v[64:67], a[136:139], v[200:203], v[64:67]\n\t"
    "ds_read_b64_tr_b16 a[166:167], v2 offset:4096\n\t"
    "v_cvt_pk_bf16_f32 v106, v14, v15\n\t"
    "v_mfma_f32_16x16x32_bf16 v[64:67], a[140:143], v[204:207], v[64:67]\n\t"
    "v_exp_f32 v16, v58\n\t"
    "v_mfma_f32_16x16x32_bf16 v[80:83], a[144:147], v[192:195], v[128:131]\n\t"
    "v_exp_f32 v17, v59\n\t"
    "v_mfma_f32_16x16x32_bf16 v[80:83], a[148:151], v[196:199], v[80:83]\n\t"
    "ds_read_b64_tr_b16 a[168:169], v1 offset:256\n\t"
    "v_cvt_pk_bf16_f32 v107, v16, v17\n\t"
    "v_mfma_f32_16x16x32_bf16 v[80:83], a[152:155], v[200:203], v[80:83]\n\t"
    "ds_read_b64_tr_b16 a[170:171], v1 offset:4352\n\t"
    "v_mfma_f32_16x16x32_bf16 v[80:83], a[156:159], v[204:207], v[80:83]\n\t"
    "v_exp_f32 v10, v44\n\t"
    "v_mfma_f32_16x16x32_bf16 v[152:155], v[176:179], v[104:107], v[168:171]\n\t"
    "v_exp_f32 v11, v45\n\t"
    "v_mfma_f32_16x16x32_bf16 v[68:71], a[128:131], v[208:211], v[132:135]\n\t"
    "ds_read_b64_tr_b16 a[172:173], v2 offset:256\n\t"
    "v_cvt_pk_bf16_f32 v108, v10, v11\n\t"
    "v_mfma_f32_16x16x32_bf16 v[68:71], a[132:135], v[212:215], v[68:71]\n\t"
    "buffer_load_dwordx4 v188, s[40:43], s58 offen offset:2048 nt lds\n\t"
    "v_mfma_f32_16x16x32_bf16 v[68:71], a[136:139], v[216:219], v[68:71]\n\t"
    "v_exp_f32 v12, v46\n\t"
    "v_mfma_f32_16x16x32_bf16 v[68:71], a[140:143], v[220:223], v[68:71]\n\t"
    "v_exp_f32 v13, v47\n\t"
    "v_mfma_f32_16x16x32_bf16 v[84:87], a[144:147], v[208:211], v[132:135]\n\t"
    "ds_read_b64_tr_b16 a[174:175], v2 offset:4352\n\t"
    "v_cvt_pk_bf16_f32 v109, v12, v13\n\t"
    "v_mfma_f32_16x16x32_bf16 v[84:87], a[148:151], v[212:215], v[84:87]\n\t"
    "ds_read_b64_tr_b16 a[176:177], v1 offset:512\n\t"
    "v_max3_f32 v9, v144, v148, v152\n\t"
    "v_mfma_f32_16x16x32_bf16 v[84:87], a[152:155], v[216:219], v[84:87]\n\t"
    "v_exp_f32 v14, v60\n\t"
    "v_mfma_f32_16x16x32_bf16 v[84:87], a[156:159], v[220:223], v[84:87]\n\t"
    "v_exp_f32 v15, v61\n\t"
    "v_mfma_f32_16x16x32_bf16 v[72:75], a[128:131], v[224:227], v[136:139]\n\t"
    "ds_read_b64_tr_b16 a[178:179], v1 offset:4608\n\t"
    "v_cvt_pk_bf16_f32 v110, v14, v15\n\t"
    "v_mfma_f32_16x16x32_bf16 v[72:75], a[132:135], v[228:231], v[72:75]\n\t"
    "v_exp_f32 v16, v62\n\t"
    "v_mfma_f32_16x16x32_bf16 v[72:75], a[136:139], v[232:235], v[72:75]\n\t"
    "v_exp_f32 v17, v63\n\t"
    "v_mfma_f32_16x16x32_bf16 v[72:75], a[140:143], v[236:239], v[72:75]\n\t"
    "ds_read_b64_tr_b16 a[180:181], v2 offset:512\n\t"
    "v_cvt_pk_bf16_f32 v111, v16, v17\n\t"
    "v_mfma_f32_16x16x32_bf16 v[88:91], a[144:147], v[224:227], v[136:139]\n\t"
    "ds_read_b64_tr_b16 a[182:183], v2 offset:4608\n\t"
    "s_cmp_eq_u32 s61, 81920\n\t"
    "v_mfma_f32_16x16x32_bf16 v[88:91], a[148:151], v[228:231], v[88:91]\n\t"
    "ds_read_b64_tr_b16 a[184:185], v1 offset:768\n\t"
    "s_cselect_b32 s61, 0, s61\n\t"
    "v_mfma_f32_16x16x32_bf16 v[156:159], v[176:179], v[108:111], v[172:175]\n\t"
    "ds_read_b64_tr_b16 a[186:187], v1 offset:4864\n\t"
    "v_add_u32 v0, s61, v3\n\t"
    "v_mfma_f32_16x16x32_bf16 v[88:91], a[152:155], v[232:235], v[88:91]\n\t"
    "ds_read_b64_tr_b16 a[188:189], v2 offset:768\n\t"
    "v_mfma_f32_16x16x32_bf16 v[88:91], a[156:159], v[236:239], v[88:91]\n\t"
    "ds_read_b64_tr_b16 a[190:191], v2 offset:4864\n\t"
    "v_mfma_f32_16x16x32_bf16 v[76:79], a[128:131], v[240:243], v[140:143]\n\t"
    "buffer_load_dwordx4 v189, s[40:43], s58 offen offset:3072 nt lds\n\t"
    "v_mfma_f32_16x16x32_bf16 v[76:79], a[132:135], v[244:247], v[76:79]\n\t"
    "s_add_u32 s58, s58, 16384\n\t"
    "s_memrealtime s[38:39]\n\t"
    "v_mfma_f32_16x16x32_bf16 v[76:79], a[136:139], v[248:251], v[76:79]\n\t"
    "v_max_f32 v9, v9, v156\n\t"
    "v_cmp_le_f32 vcc, s63, v9\n\t"
    "v_mfma_f32_16x16x32_bf16 v[76:79], a[140:143], v[252:255], v[76:79]\n\t"
    "v_mfma_f32_16x16x32_bf16 v[92:95], a[144:147], v[240:243], v[140:143]\n\t"
    "s_cbranch_vccz L_ret0p4_%=\n\t"
    "s_swappc_b64 s[34:35], s[30:31]\n\t"
    "L_ret0p4_%=:\n\t"
    "v_mfma_f32_16x16x32_bf16 v[92:95], a[148:151], v[244:247], v[92:95]\n\t"
    "s_waitcnt lgkmcnt(0)\n\t"
    "v_mfma_f32_16x16x32_bf16 v[92:95], a[152:155], v[248:251], v[92:95]\n\t"
    "s_sub_u32 s37, s36, s38\n\t"
    "s_cmp_gt_i32 s37, 0\n\t"
    "v_mfma_f32_16x16x32_bf16 v[92:95], a[156:159], v[252:255], v[92:95]\n\t"
    "s_cbranch_scc1 L_wait0p4_%=\n\t"
    "L_back0p4_%=:\n\t"
    "s_add_u32 s36, s38, 130\n\t"
    "s_sleep 1\n\t"
    "v_mfma_f32_16x16x32_bf16 a[0:3], a[160:163], v[96:99], a[0:3]\n\t"
    "v_exp_f32 v10, v64\n\t"
    "v_mfma_f32_16x16x32_bf16 a[16:19], a[164:167], v[96:99], a[16:19]\n\t"
    "v_exp_f32 v11, v65\n\t"
    "v_mfma_f32_16x16x32_bf16 a[32:35], a[168:171], v[96:99], a[32:35]\n\t"
    "ds_read_b128 a[128:131], v0 offset:0\n\t"
    "v_cvt_pk_bf16_f32 v112, v10, v11\n\t"
    "v_mfma_f32_16x16x32_bf16 a[48:51], a[172:175], v[96:99], a[48:51]\n\t"
    "v_exp_f32 v12, v66\n\t"
    "v_mfma_f32_16x16x32_bf16 a[64:67], a[176:179], v[96:99], a[64:67]\n\t"
    "v_exp_f32 v13, v67\n\t"
    "v_mfma_f32_16x16x32_bf16 a[80:83], a[180:183], v[96:99], a[80:83]\n\t"
    "ds_read_b128 a[132:135], v0 offset:256\n\t"
    "v_cvt_pk_bf16_f32 v113, v12, v13\n\t"
    "v_mfma_f32_16x16x32_bf16 a[96:99], a[184:187], v[96:99], a[96:99]\n\t"
    "s_add_u32 m0, s59, s57\n\t"
    "s_mov_b32 s59, s60\n\t"
    "v_mfma_f32_16x16x32_bf16 a[112:115], a[188:191], v[96:99], a[112:115]\n\t"
    "v_exp_f32 v14, v80\n\t"
    "v_mfma_f32_16x16x32_bf16 a[4:7], a[160:163], v[100:103], a[4:7]\n\t"
    "v_exp_f32 v15, v81\n\t"
    "v_mfma_f32_16x16x32_bf16 a[20:23], a[164:167], v[100:103], a[20:23]\n\t"
    "ds_read_b128 a[136:139], v0 offset:512\n\t"
    "v_cvt_pk_bf16_f32 v114, v14, v15\n\t"
    "v_mfma_f32_16x16x32_bf16 a[36:39], a[168:171], v[100:103], a[36:39]\n\t"
    "buffer_load_dwordx4 v8, s[44:47], s58 offen nt lds\n\t"
    "v_mfma_f32_16x16x32_bf16 a[52:55], a[172:175], v[100:103], a[52:55]\n\t"
    "v_exp_f32 v16, v82\n\t"
    "v_mfma_f32_16x16x32_bf16 a[68:71], a[176:179], v[100:103], a[68:71]\n\t"
    "v_exp_f32 v17, v83\n\t"
    "v_mfma_f32_16x16x32_bf16 a[84:87], a[180:183], v[100:103], a[84:87]\n\t"
    "ds_read_b128 a[140:143], v0 offset:768\n\t"
    "v_cvt_pk_bf16_f32 v115, v16, v17\n\t"
    "v_mfma_f32_16x16x32_bf16 a[100:103], a[184:187], v[100:103], a[100:103]\n\t"
    "v_exp_f32 v10, v68\n\t"
    "v_mfma_f32_16x16x32_bf16 a[116:119], a[188:191], v[100:103], a[116:119]\n\t"
    "v_exp_f32 v11, v69\n\t"
    "v_mfma_f32_16x16x32_bf16 v[160:163], v[176:179], v[112:115], v[144:147]\n\t"
    "ds_read_b128 a[144:147], v0 offset:4096\n\t"
    "v_cvt_pk_bf16_f32 v116, v10, v11\n\t"
    "v_mfma_f32_16x16x32_bf16 a[8:11], a[160:163], v[104:107], a[8:11]\n\t"
    "v_exp_f32 v12, v70\n\t"
    "v_mfma_f32_16x16x32_bf16 a[24:27], a[164:167], v[104:107], a[24:27]\n\t"
    "v_exp_f32 v13, v71\n\t"
    "v_mfma_f32_16x16x32_bf16 a[40:43], a[168:171], v[104:107], a[40:43]\n\t"
    "ds_read_b128 a[148:151], v0 offset:4352\n\t"
    "v_cvt_pk_bf16_f32 v117, v12, v13\n\t"
    "v_mfma_f32_16x16x32_bf16 a[56:59], a[172:175], v[104:107], a[56:59]\n\t"
    "v_exp_f32 v14, v84\n\t"
    "v_mfma_f32_16x16x32_bf16 a[72:75], a[176:179], v[104:107], a[72:75]\n\t"
    "v_exp_f32 v15, v85\n\t"
    "v_mfma_f32_16x16x32_bf16 a[88:91], a[180:183], v[104:107], a[88:91]\n\t"
    "ds_read_b128 a[152:155], v0 offset:4608\n\t"
    "v_cvt_pk_bf16_f32 v118, v14, v15\n\t"
    "v_mfma_f32_16x16x32_bf16 a[104:107], a[184:187], v[104:107], a[104:107]\n\t"
    "v_exp_f32 v16, v86\n\t"
    "v_mfma_f32_16x16x32_bf16 a[120:123], a[188:191], v[104:107], a[120:123]\n\t"
    "v_exp_f32 v17, v87\n\t"
    "v_mfma_f32_16x16x32_bf16 a[12:15], a[160:163], v[108:111], a[12:15]\n\t"
    "ds_read_b128 a[156:159], v0 offset:4864\n\t"
    "v_cvt_pk_bf16_f32 v119, v16, v17\n\t"
    "v_mfma_f32_16x16x32_bf16 a[28:31], a[164:167], v[108:111], a[28:31]\n\t"
    "buffer_load_dwordx4 v190, s[44:47], s58 offen offset:1024 nt lds\n\t"
    "v_mfma_f32_16x16x32_bf16 a[44:47], a[168:171], v[108:111], a[44:47]\n\t"
    "v_exp_f32 v10, v72\n\t"
    "v_mfma_f32_16x16x32_bf16 v[164:167], v[176:179], v[116:119], v[148:151]\n\t"
    "v_exp_f32 v11, v73\n\t"
    "v_mfma_f32_16x16x32_bf16 a[60:63], a[172:175], v[108:111], a[60:63]\n\t"
    "ds_read_b64_tr_b16 a[160:161], v1 offset:8192\n\t"
    "v_cvt_pk_bf16_f32 v120, v10, v11\n\t"
    "v_mfma_f32_16x16x32_bf16 a[76:79], a[176:179], v[108:111], a[76:79]\n\t"
    "v_exp_f32 v12, v74\n\t"
    "v_mfma_f32_16x16x32_bf16 a[92:95], a[180:183], v[108:111], a[92:95]\n\t"
    "v_exp_f32 v13, v75\n\t"
    "v_mfma_f32_16x16x32_bf16 a[108:111], a[184:187], v[108:111], a[108:111]\n\t"
    "ds_read_b64_tr_b16 a[162:163], v1 offset:12288\n\t"
    "v_cvt_pk_bf16_f32 v121, v12, v13\n\t"
    "v_mfma_f32_16x16x32_bf16 a[124:127], a[188:191], v[108:111], a[124:127]\n\t"
    "ds_read_b64_tr_b16 a[164:165], v2 offset:8192\n\t"
    "s_waitcnt lgkmcnt(3)\n\t"
    "v_mfma_f32_16x16x32_bf16 v[32:35], a[128:131], v[192:195], v[128:131]\n\t"
    "v_exp_f32 v14, v88\n\t"
    "v_mfma_f32_16x16x32_bf16 v[32:35], a[132:135], v[196:199], v[32:35]\n\t"
    "v_exp_f32 v15, v89\n\t"
    "v_mfma_f32_16x16x32_bf16 v[32:35], a[136:139], v[200:203], v[32:35]\n\t"
    "ds_read_b64_tr_b16 a[166:167], v2 offset:12288\n\t"
    "v_cvt_pk_bf16_f32 v122, v14, v15\n\t"
    "v_mfma_f32_16x16x32_bf16 v[32:35], a[140:143], v[204:207], v[32:35]\n\t"
    "v_exp_f32 v16, v90\n\t"
    "v_mfma_f32_16x16x32_bf16 v[48:51], a[144:147], v[192:195], v[128:131]\n\t"
    "v_exp_f32 v17, v91\n\t"
    "v_mfma_f32_16x16x32_bf16 v[48:51], a[148:151], v[196:199], v[48:51]\n\t"
    "ds_read_b64_tr_b16 a[168:169], v1 offset:8448\n\t"
    "v_cvt_pk_bf16_f32 v123, v16, v17\n\t"
    "v_mfma_f32_16x16x32_bf16 v[48:51], a[152:155], v[200:203], v[48:51]\n\t"
    "ds_read_b64_tr_b16 a[170:171], v1 offset:12544\n\t"
    "v_mfma_f32_16x16x32_bf16 v[48:51], a[156:159], v[204:207], v[48:51]\n\t"
    "v_exp_f32 v10, v76\n\t"
    "v_mfma_f32_16x16x32_bf16 v[168:171], v[176:179], v[120:123], v[152:155]\n\t"
    "v_exp_f32 v11, v77\n\t"
    "v_mfma_f32_16x16x32_bf16 v[36:39], a[128:131], v[208:211], v[132:135]\n\t"
    "ds_read_b64_tr_b16 a[172:173], v2 offset:8448\n\t"
    "v_cvt_pk_bf16_f32 v124, v10, v11\n\t"
    "v_mfma_f32_16x16x32_bf16 v[36:39], a[132:135], v[212:215], v[36:39]\n\t"
    "buffer_load_dwordx4 v8, s[44:47], s58 offen offset:2048 nt lds\n\t"
    "v_mfma_f32_16x16x32_bf16 v[36:39], a[136:139], v[216:219], v[36:39]\n\t"
    "v_exp_f32 v12, v78\n\t"
    "v_mfma_f32_16x16x32_bf16 v[36:39], a[140:143], v[220:223], v[36:39]\n\t"
    "v_exp_f32 v13, v79\n\t"
    "v_mfma_f32_16x16x32_bf16 v[52:55], a[144:147], v[208:211], v[132:135]\n\t"
    "ds_read_b64_tr_b16 a[174:175], v2 offset:12544\n\t"
    "v_cvt_pk_bf16_f32 v125, v12, v13\n\t"
    "v_mfma_f32_16x16x32_bf16 v[52:55], a[148:151], v[212:215], v[52:55]\n\t"
    "ds_read_b64_tr_b16 a[176:177], v1 offset:8704\n\t"
    "v_max3_f32 v9, v160, v164, v168\n\t"
    "v_mfma_f32_16x16x32_bf16 v[52:55], a[152:155], v[216:219], v[52:55]\n\t"
    "v_exp_f32 v14, v92\n\t"
    "v_mfma_f32_16x16x32_bf16 v[52:55], a[156:159], v[220:223], v[52:55]\n\t"
    "v_exp_f32 v15, v93\n\t"
    "v_mfma_f32_16x16x32_bf16 v[40:43], a[128:131], v[224:227], v[136:139]\n\t"
    "ds_read_b64_tr_b16 a[178:179], v1 offset:12800\n\t"
    "v_cvt_pk_bf16_f32 v126, v14, v15\n\t"
    "v_mfma_f32_16x16x32_bf16 v[40:43], a[132:135], v[228:231], v[40:43]\n\t"
    "v_exp_f32 v16, v94\n\t"
    "v_mfma_f32_16x16x32_bf16 v[40:43], a[136:139], v[232:235], v[40:43]\n\t"
    "v_exp_f32 v17, v95\n\t"
    "v_mfma_f32_16x16x32_bf16 v[40:43], a[140:143], v[236:239], v[40:43]\n\t"
    "ds_read_b64_tr_b16 a[180:181], v2 offset:8704\n\t"
    "v_cvt_pk_bf16_f32 v127, v16, v17\n\t"
    "v_mfma_f32_16x16x32_bf16 v[56:59], a[144:147], v[224:227], v[136:139]\n\t"
    "ds_read_b64_tr_b16 a[182:183], v2 offset:12800\n\t"
    "s_mov_b32 s60, s61\n\t"
    "v_mfma_f32_16x16x32_bf16 v[56:59], a[148:151], v[228:231], v[56:59]\n\t"
    "ds_read_b64_tr_b16 a[184:185], v1 offset:8960\n\t"
    "v_mfma_f32_16x16x32_bf16 v[172:175], v[176:179], v[124:127], v[156:159]\n\t"
    "ds_read_b64_tr_b16 a[186:187], v1 offset:13056\n\t"
    "v_mfma_f32_16x16x32_bf16 v[56:59], a[152:155], v[232:235], v[56:59]\n\t"
    "ds_read_b64_tr_b16 a[188:189], v2 offset:8960\n\t"
    "v_mfma_f32_16x16x32_bf16 v[56:59], a[156:159], v[236:239], v[56:59]\n\t"
    "ds_read_b64_tr_b16 a[190:191], v2 offset:13056\n\t"
    "v_mfma_f32_16x16x32_bf16 v[44:47], a[128:131], v[240:243], v[140:143]\n\t"
    "buffer_load_dwordx4 v190, s[44:47], s58 offen offset:3072 nt lds\n\t"
    "v_mfma_f32_16x16x32_bf16 v[44:47], a[132:135], v[244:247], v[44:47]\n\t"
    "v_add_u32 v1, s60, v4\n\t"
    "v_add_u32 v2, s60, v5\n\t"
    "v_mfma_f32_16x16x32_bf16 v[44:47], a[136:139], v[248:251], v[44:47]\n\t"
    "v_max_f32 v9, v9, v172\n\t"
    "v_cmp_le_f32 vcc, s63, v9\n\t"
    "v_mfma_f32_16x16x32_bf16 v[44:47], a[140:143], v[252:255], v[44:47]\n\t"
    "v_mfma_f32_16x16x32_bf16 v[60:63], a[144:147], v[240:243], v[140:143]\n\t"
    "s_cbranch_vccz L_ret1p5_%=\n\t"
    "s_swappc_b64 s[34:35], s[32:33]\n\t"
    "L_ret1p5_%=:\n\t"
    "v_mfma_f32_16x16x32_bf16 v[60:63], a[148:151], v[244:247], v[60:63]\n\t"
    "s_waitcnt lgkmcnt(0)\n\t"
    "v_mfma_f32_16x16x32_bf16 v[60:63], a[152:155], v[248:251], v[60:63]\n\t"
    "s_waitcnt vmcnt(20)\n\t"
    "s_barrier\n\t"
    "v_mfma_f32_16x16x32_bf16 v[60:63], a[156:159], v[252:255], v[60:63]\n\t"
    "s_sleep 1\n\t"
    "s_sub_u32 s62, s62, 1\n\t"
    "v_mfma_f32_16x16x32_bf16 a[0:3], a[160:163], v[112:115], a[0:3]\n\t"
    "v_exp_f32 v10, v32\n\t"
    "v_mfma_f32_16x16x32_bf16 a[16:19], a[164:167], v[112:115], a[16:19]\n\t"
    "v_exp_f32 v11, v33\n\t"
    "v_mfma_f32_16x16x32_bf16 a[32:35], a[168:171], v[112:115], a[32:35]\n\t"
    "ds_read_b128 a[128:131], v0 offset:8192\n\t"
    "v_cvt_pk_bf16_f32 v96, v10, v11\n\t"
    "v_mfma_f32_16x16x32_bf16 a[48:51], a[172:175], v[112:115], a[48:51]\n\t"
    "v_exp_f32 v12, v34\n\t"
    "v_mfma_f32_16x16x32_bf16 a[64:67], a[176:179], v[112:115], a[64:67]\n\t"
    "v_exp_f32 v13, v35\n\t"
    "v_mfma_f32_16x16x32_bf16 a[80:83], a[180:183], v[112:115], a[80:83]\n\t"
    "ds_read_b128 a[132:135], v0 offset:8448\n\t"
    "v_cvt_pk_bf16_f32 v97, v12, v13\n\t"
    "v_mfma_f32_16x16x32_bf16 a[96:99], a[184:187], v[112:115], a[96:99]\n\t"
    "s_add_u32 m0, s59, s56\n\t"
    "s_add_u32 s61, s61, 16384\n\t"
    "v_mfma_f32_16x16x32_bf16 a[112:115], a[188:191], v[112:115], a[112:115]\n\t"
    "v_exp_f32 v14, v48\n\t"
    "v_mfma_f32_16x16x32_bf16 a[4:7], a[160:163], v[116:119], a[4:7]\n\t"
    "v_exp_f32 v15, v49\n\t"
    "v_mfma_f32_16x16x32_bf16 a[20:23], a[164:167], v[116:119], a[20:23]\n\t"
    "ds_read_b128 a[136:139], v0 offset:8704\n\t"
    "v_cvt_pk_bf16_f32 v98, v14, v15\n\t"
    "v_mfma_f32_16x16x32_bf16 a[36:39], a[168:171], v[116:119], a[36:39]\n\t"
    "buffer_load_dwordx4 v6, s[40:43], s58 offen nt lds\n\t"
    "v_mfma_f32_16x16x32_bf16 a[52:55], a[172:175], v[116:119], a[52:55]\n\t"
    "v_exp_f32 v16, v50\n\t"
    "v_mfma_f32_16x16x32_bf16 a[68:71], a[176:179], v[116:119], a[68:71]\n\t"
    "v_exp_f32 v17, v51\n\t"
    "v_mfma_f32_16x16x32_bf16 a[84:87], a[180:183], v[116:119], a[84:87]\n\t"
    "ds_read_b128 a[140:143], v0 offset:8960\n\t"
    "v_cvt_pk_bf16_f32 v99, v16, v17\n\t"
    "v_mfma_f32_16x16x32_bf16 a[100:103], a[184:187], v[116:119], a[100:103]\n\t"
    "v_exp_f32 v10, v36\n\t"
    "v_mfma_f32_16x16x32_bf16 a[116:119], a[188:191], v[116:119], a[116:119]\n\t"
    "v_exp_f32 v11, v37\n\t"
    "v_mfma_f32_16x16x32_bf16 v[144:147], v[176:179], v[96:99], v[160:163]\n\t"
    "ds_read_b128 a[144:147], v0 offset:12288\n\t"
    "v_cvt_pk_bf16_f32 v100, v10, v11\n\t"
    "v_mfma_f32_16x16x32_bf16 a[8:11], a[160:163], v[120:123], a[8:11]\n\t"
    "v_exp_f32 v12, v38\n\t"
    "v_mfma_f32_16x16x32_bf16 a[24:27], a[164:167], v[120:123], a[24:27]\n\t"
    "v_exp_f32 v13, v39\n\t"
    "v_mfma_f32_16x16x32_bf16 a[40:43], a[168:171], v[120:123], a[40:43]\n\t"
    "ds_read_b128 a[148:151], v0 offset:12544\n\t"
    "v_cvt_pk_bf16_f32 v101, v12, v13\n\t"
    "v_mfma_f32_16x16x32_bf16 a[56:59], a[172:175], v[120:123], a[56:59]\n\t"
    "v_exp_f32 v14, v52\n\t"
    "v_mfma_f32_16x16x32_bf16 a[72:75], a[176:179], v[120:123], a[72:75]\n\t"
    "v_exp_f32 v15, v53\n\t"
    "v_mfma_f32_16x16x32_bf16 a[88:91], a[180:183], v[120:123], a[88:91]\n\t"
    "ds_read_b128 a[152:155], v0 offset:12800\n\t"
    "v_cvt_pk_bf16_f32 v102, v14, v15\n\t"
    "v_mfma_f32_16x16x32_bf16 a[104:107], a[184:187], v[120:123], a[104:107]\n\t"
    "v_exp_f32 v16, v54\n\t"
    "v_mfma_f32_16x16x32_bf16 a[120:123], a[188:191], v[120:123], a[120:123]\n\t"
    "v_exp_f32 v17, v55\n\t"
    "v_mfma_f32_16x16x32_bf16 a[12:15], a[160:163], v[124:127], a[12:15]\n\t"
    "ds_read_b128 a[156:159], v0 offset:13056\n\t"
    "v_cvt_pk_bf16_f32 v103, v16, v17\n\t"
    "v_mfma_f32_16x16x32_bf16 a[28:31], a[164:167], v[124:127], a[28:31]\n\t"
    "buffer_load_dwordx4 v7, s[40:43], s58 offen offset:1024 nt lds\n\t"
    "v_mfma_f32_16x16x32_bf16 a[44:47], a[168:171], v[124:127], a[44:47]\n\t"
    "v_exp_f32 v10, v40\n\t"
    "v_mfma_f32_16x16x32_bf16 v[148:151], v[176:179], v[100:103], v[164:167]\n\t"
    "v_exp_f32 v11, v41\n\t"
    "v_mfma_f32_16x16x32_bf16 a[60:63], a[172:175], v[124:127], a[60:63]\n\t"
    "ds_read_b64_tr_b16 a[160:161], v1 offset:0\n\t"
    "v_cvt_pk_bf16_f32 v104, v10, v11\n\t"
    "v_mfma_f32_16x16x32_bf16 a[76:79], a[176:179], v[124:127], a[76:79]\n\t"
    "v_exp_f32 v12, v42\n\t"
    "v_mfma_f32_16x16x32_bf16 a[92:95], a[180:183], v[124:127], a[92:95]\n\t"
    "v_exp_f32 v13, v43\n\t"
    "v_mfma_f32_16x16x32_bf16 a[108:111], a[184:187], v[124:127], a[108:111]\n\t"
    "ds_read_b64_tr_b16 a[162:163], v1 offset:4096\n\t"
    "v_cvt_pk_bf16_f32 v105, v12, v13\n\t"
    "v_mfma_f32_16x16x32_bf16 a[124:127], a[188:191], v[124:127], a[124:127]\n\t"
    "ds_read_b64_tr_b16 a[164:165], v2 offset:0\n\t"
    "s_waitcnt lgkmcnt(3)\n\t"
    "v_mfma_f32_16x16x32_bf16 v[64:67], a[128:131], v[192:195], v[128:131]\n\t"
    "v_exp_f32 v14, v56\n\t"
    "v_mfma_f32_16x16x32_bf16 v[64:67], a[132:135], v[196:199], v[64:67]\n\t"
    "v_exp_f32 v15, v57\n\t"
    "v_mfma_f32_16x16x32_bf16 v[64:67], a[136:139], v[200:203], v[64:67]\n\t"
    "ds_read_b64_tr_b16 a[166:167], v2 offset:4096\n\t"
    "v_cvt_pk_bf16_f32 v106, v14, v15\n\t"
    "v_mfma_f32_16x16x32_bf16 v[64:67], a[140:143], v[204:207], v[64:67]\n\t"
    "v_exp_f32 v16, v58\n\t"
    "v_mfma_f32_16x16x32_bf16 v[80:83], a[144:147], v[192:195], v[128:131]\n\t"
    "v_exp_f32 v17, v59\n\t"
    "v_mfma_f32_16x16x32_bf16 v[80:83], a[148:151], v[196:199], v[80:83]\n\t"
    "ds_read_b64_tr_b16 a[168:169], v1 offset:256\n\t"
    "v_cvt_pk_bf16_f32 v107, v16, v17\n\t"
    "v_mfma_f32_16x16x32_bf16 v[80:83], a[152:155], v[200:203], v[80:83]\n\t"
    "ds_read_b64_tr_b16 a[170:171], v1 offset:4352\n\t"
    "v_mfma_f32_16x16x32_bf16 v[80:83], a[156:159], v[204:207], v[80:83]\n\t"
    "v_exp_f32 v10, v44\n\t"
    "v_mfma_f32_16x16x32_bf16 v[152:155], v[176:179], v[104:107], v[168:171]\n\t"
    "v_exp_f32 v11, v45\n\t"
    "v_mfma_f32_16x16x32_bf16 v[68:71], a[128:131], v[208:211], v[132:135]\n\t"
    "ds_read_b64_tr_b16 a[172:173], v2 offset:256\n\t"
    "v_cvt_pk_bf16_f32 v108, v10, v11\n\t"
    "v_mfma_f32_16x16x32_bf16 v[68:71], a[132:135], v[212:215], v[68:71]\n\t"
    "buffer_load_dwordx4 v188, s[40:43], s58 offen offset:2048 nt lds\n\t"
    "v_mfma_f32_16x16x32_bf16 v[68:71], a[136:139], v[216:219], v[68:71]\n\t"
    "v_exp_f32 v12, v46\n\t"
    "v_mfma_f32_16x16x32_bf16 v[68:71], a[140:143], v[220:223], v[68:71]\n\t"
    "v_exp_f32 v13, v47\n\t"
    "v_mfma_f32_16x16x32_bf16 v[84:87], a[144:147], v[208:211], v[132:135]\n\t"
    "ds_read_b64_tr_b16 a[174:175], v2 offset:4352\n\t"
    "v_cvt_pk_bf16_f32 v109, v12, v13\n\t"
    "v_mfma_f32_16x16x32_bf16 v[84:87], a[148:151], v[212:215], v[84:87]\n\t"
    "ds_read_b64_tr_b16 a[176:177], v1 offset:512\n\t"
    "v_max3_f32 v9, v144, v148, v152\n\t"
    "v_mfma_f32_16x16x32_bf16 v[84:87], a[152:155], v[216:219], v[84:87]\n\t"
    "v_exp_f32 v14, v60\n\t"
    "v_mfma_f32_16x16x32_bf16 v[84:87], a[156:159], v[220:223], v[84:87]\n\t"
    "v_exp_f32 v15, v61\n\t"
    "v_mfma_f32_16x16x32_bf16 v[72:75], a[128:131], v[224:227], v[136:139]\n\t"
    "ds_read_b64_tr_b16 a[178:179], v1 offset:4608\n\t"
    "v_cvt_pk_bf16_f32 v110, v14, v15\n\t"
    "v_mfma_f32_16x16x32_bf16 v[72:75], a[132:135], v[228:231], v[72:75]\n\t"
    "v_exp_f32 v16, v62\n\t"
    "v_mfma_f32_16x16x32_bf16 v[72:75], a[136:139], v[232:235], v[72:75]\n\t"
    "v_exp_f32 v17, v63\n\t"
    "v_mfma_f32_16x16x32_bf16 v[72:75], a[140:143], v[236:239], v[72:75]\n\t"
    "ds_read_b64_tr_b16 a[180:181], v2 offset:512\n\t"
    "v_cvt_pk_bf16_f32 v111, v16, v17\n\t"
    "v_mfma_f32_16x16x32_bf16 v[88:91], a[144:147], v[224:227], v[136:139]\n\t"
    "ds_read_b64_tr_b16 a[182:183], v2 offset:4608\n\t"
    "s_cmp_eq_u32 s61, 81920\n\t"
    "v_mfma_f32_16x16x32_bf16 v[88:91], a[148:151], v[228:231], v[88:91]\n\t"
    "ds_read_b64_tr_b16 a[184:185], v1 offset:768\n\t"
    "s_cselect_b32 s61, 0, s61\n\t"
    "v_mfma_f32_16x16x32_bf16 v[156:159], v[176:179], v[108:111], v[172:175]\n\t"
    "ds_read_b64_tr_b16 a[186:187], v1 offset:4864\n\t"
    "v_add_u32 v0, s61, v3\n\t"
    "v_mfma_f32_16x16x32_bf16 v[88:91], a[152:155], v[232:235], v[88:91]\n\t"
    "ds_read_b64_tr_b16 a[188:189], v2 offset:768\n\t"
    "v_mfma_f32_16x16x32_bf16 v[88:91], a[156:159], v[236:239], v[88:91]\n\t"
    "ds_read_b64_tr_b16 a[190:191], v2 offset:4864\n\t"
    "v_mfma_f32_16x16x32_bf16 v[76:79], a[128:131], v[240:243], v[140:143]\n\t"
    "buffer_load_dwordx4 v189, s[40:43], s58 offen offset:3072 nt lds\n\t"
    "v_mfma_f32_16x16x32_bf16 v[76:79], a[132:135], v[244:247], v[76:79]\n\t"
    "s_add_u32 s58, s58, 16384\n\t"
    "s_memrealtime s[38:39]\n\t"
    "v_mfma_f32_16x16x32_bf16 v[76:79], a[136:139], v[248:251], v[76:79]\n\t"
    "v_max_f32 v9, v9, v156\n\t"
    "v_cmp_le_f32 vcc, s63, v9\n\t"
    "v_mfma_f32_16x16x32_bf16 v[76:79], a[140:143], v[252:255], v[76:79]\n\t"
    "v_mfma_f32_16x16x32_bf16 v[92:95], a[144:147], v[240:243], v[140:143]\n\t"
    "s_cbranch_vccz L_ret0p6_%=\n\t"
    "s_swappc_b64 s[34:35], s[30:31]\n\t"
    "L_ret0p6_%=:\n\t"
    "v_mfma_f32_16x16x32_bf16 v[92:95], a[148:151], v[244:247], v[92:95]\n\t"
    "s_waitcnt lgkmcnt(0)\n\t"
    "v_mfma_f32_16x16x32_bf16 v[92:95], a[152:155], v[248:251], v[92:95]\n\t"
    "s_sub_u32 s37, s36, s38\n\t"
    "s_cmp_gt_i32 s37, 0\n\t"
    "v_mfma_f32_16x16x32_bf16 v[92:95], a[156:159], v[252:255], v[92:95]\n\t"
    "s_cbranch_scc1 L_wait0p6_%=\n\t"
    "L_back0p6_%=:\n\t"
    "s_add_u32 s36, s38, 130\n\t"
    "s_sleep 1\n\t"
    "v_mfma_f32_16x16x32_bf16 a[0:3], a[160:163], v[96:99], a[0:3]\n\t"
    "v_exp_f32 v10, v64\n\t"
    "v_mfma_f32_16x16x32_bf16 a[16:19], a[164:167], v[96:99], a[16:19]\n\t"
    "v_exp_f32 v11, v65\n\t"
    "v_mfma_f32_16x16x32_bf16 a[32:35], a[168:171], v[96:99], a[32:35]\n\t"
    "ds_read_b128 a[128:131], v0 offset:0\n\t"
    "v_cvt_pk_bf16_f32 v112, v10, v11\n\t"
    "v_mfma_f32_16x16x32_bf16 a[48:51], a[172:175], v[96:99], a[48:51]\n\t"
    "v_exp_f32 v12, v66\n\t"
    "v_mfma_f32_16x16x32_bf16 a[64:67], a[176:179], v[96:99], a[64:67]\n\t"
    "v_exp_f32 v13, v67\n\t"
    "v_mfma_f32_16x16x32_bf16 a[80:83], a[180:183], v[96:99], a[80:83]\n\t"
    "ds_read_b128 a[132:135], v0 offset:256\n\t"
    "v_cvt_pk_bf16_f32 v113, v12, v13\n\t"
    "v_mfma_f32_16x16x32_bf16 a[96:99], a[184:187], v[96:99], a[96:99]\n\t"
    "s_add_u32 m0, s59, s57\n\t"
    "s_mov_b32 s59, s60\n\t"
    "v_mfma_f32_16x16x32_bf16 a[112:115], a[188:191], v[96:99], a[112:115]\n\t"
    "v_exp_f32 v14, v80\n\t"
    "v_mfma_f32_16x16x32_bf16 a[4:7], a[160:163], v[100:103], a[4:7]\n\t"
    "v_exp_f32 v15, v81\n\t"
    "v_mfma_f32_16x16x32_bf16 a[20:23], a[164:167], v[100:103], a[20:23]\n\t"
    "ds_read_b128 a[136:139], v0 offset:512\n\t"
    "v_cvt_pk_bf16_f32 v114, v14, v15\n\t"
    "v_mfma_f32_16x16x32_bf16 a[36:39], a[168:171], v[100:103], a[36:39]\n\t"
    "buffer_load_dwordx4 v8, s[44:47], s58 offen nt lds\n\t"
    "v_mfma_f32_16x16x32_bf16 a[52:55], a[172:175], v[100:103], a[52:55]\n\t"
    "v_exp_f32 v16, v82\n\t"
    "v_mfma_f32_16x16x32_bf16 a[68:71], a[176:179], v[100:103], a[68:71]\n\t"
    "v_exp_f32 v17, v83\n\t"
    "v_mfma_f32_16x16x32_bf16 a[84:87], a[180:183], v[100:103], a[84:87]\n\t"
    "ds_read_b128 a[140:143], v0 offset:768\n\t"
    "v_cvt_pk_bf16_f32 v115, v16, v17\n\t"
    "v_mfma_f32_16x16x32_bf16 a[100:103], a[184:187], v[100:103], a[100:103]\n\t"
    "v_exp_f32 v10, v68\n\t"
    "v_mfma_f32_16x16x32_bf16 a[116:119], a[188:191], v[100:103], a[116:119]\n\t"
    "v_exp_f32 v11, v69\n\t"
    "v_mfma_f32_16x16x32_bf16 v[160:163], v[176:179], v[112:115], v[144:147]\n\t"
    "ds_read_b128 a[144:147], v0 offset:4096\n\t"
    "v_cvt_pk_bf16_f32 v116, v10, v11\n\t"
    "v_mfma_f32_16x16x32_bf16 a[8:11], a[160:163], v[104:107], a[8:11]\n\t"
    "v_exp_f32 v12, v70\n\t"
    "v_mfma_f32_16x16x32_bf16 a[24:27], a[164:167], v[104:107], a[24:27]\n\t"
    "v_exp_f32 v13, v71\n\t"
    "v_mfma_f32_16x16x32_bf16 a[40:43], a[168:171], v[104:107], a[40:43]\n\t"
    "ds_read_b128 a[148:151], v0 offset:4352\n\t"
    "v_cvt_pk_bf16_f32 v117, v12, v13\n\t"
    "v_mfma_f32_16x16x32_bf16 a[56:59], a[172:175], v[104:107], a[56:59]\n\t"
    "v_exp_f32 v14, v84\n\t"
    "v_mfma_f32_16x16x32_bf16 a[72:75], a[176:179], v[104:107], a[72:75]\n\t"
    "v_exp_f32 v15, v85\n\t"
    "v_mfma_f32_16x16x32_bf16 a[88:91], a[180:183], v[104:107], a[88:91]\n\t"
    "ds_read_b128 a[152:155], v0 offset:4608\n\t"
    "v_cvt_pk_bf16_f32 v118, v14, v15\n\t"
    "v_mfma_f32_16x16x32_bf16 a[104:107], a[184:187], v[104:107], a[104:107]\n\t"
    "v_exp_f32 v16, v86\n\t"
    "v_mfma_f32_16x16x32_bf16 a[120:123], a[188:191], v[104:107], a[120:123]\n\t"
    "v_exp_f32 v17, v87\n\t"
    "v_mfma_f32_16x16x32_bf16 a[12:15], a[160:163], v[108:111], a[12:15]\n\t"
    "ds_read_b128 a[156:159], v0 offset:4864\n\t"
    "v_cvt_pk_bf16_f32 v119, v16, v17\n\t"
    "v_mfma_f32_16x16x32_bf16 a[28:31], a[164:167], v[108:111], a[28:31]\n\t"
    "buffer_load_dwordx4 v190, s[44:47], s58 offen offset:1024 nt lds\n\t"
    "v_mfma_f32_16x16x32_bf16 a[44:47], a[168:171], v[108:111], a[44:47]\n\t"
    "v_exp_f32 v10, v72\n\t"
    "v_mfma_f32_16x16x32_bf16 v[164:167], v[176:179], v[116:119], v[148:151]\n\t"
    "v_exp_f32 v11, v73\n\t"
    "v_mfma_f32_16x16x32_bf16 a[60:63], a[172:175], v[108:111], a[60:63]\n\t"
    "ds_read_b64_tr_b16 a[160:161], v1 offset:8192\n\t"
    "v_cvt_pk_bf16_f32 v120, v10, v11\n\t"
    "v_mfma_f32_16x16x32_bf16 a[76:79], a[176:179], v[108:111], a[76:79]\n\t"
    "v_exp_f32 v12, v74\n\t"
    "v_mfma_f32_16x16x32_bf16 a[92:95], a[180:183], v[108:111], a[92:95]\n\t"
    "v_exp_f32 v13, v75\n\t"
    "v_mfma_f32_16x16x32_bf16 a[108:111], a[184:187], v[108:111], a[108:111]\n\t"
    "ds_read_b64_tr_b16 a[162:163], v1 offset:12288\n\t"
    "v_cvt_pk_bf16_f32 v121, v12, v13\n\t"
    "v_mfma_f32_16x16x32_bf16 a[124:127], a[188:191], v[108:111], a[124:127]\n\t"
    "ds_read_b64_tr_b16 a[164:165], v2 offset:8192\n\t"
    "s_waitcnt lgkmcnt(3)\n\t"
    "v_mfma_f32_16x16x32_bf16 v[32:35], a[128:131], v[192:195], v[128:131]\n\t"
    "v_exp_f32 v14, v88\n\t"
    "v_mfma_f32_16x16x32_bf16 v[32:35], a[132:135], v[196:199], v[32:35]\n\t"
    "v_exp_f32 v15, v89\n\t"
    "v_mfma_f32_16x16x32_bf16 v[32:35], a[136:139], v[200:203], v[32:35]\n\t"
    "ds_read_b64_tr_b16 a[166:167], v2 offset:12288\n\t"
    "v_cvt_pk_bf16_f32 v122, v14, v15\n\t"
    "v_mfma_f32_16x16x32_bf16 v[32:35], a[140:143], v[204:207], v[32:35]\n\t"
    "v_exp_f32 v16, v90\n\t"
    "v_mfma_f32_16x16x32_bf16 v[48:51], a[144:147], v[192:195], v[128:131]\n\t"
    "v_exp_f32 v17, v91\n\t"
    "v_mfma_f32_16x16x32_bf16 v[48:51], a[148:151], v[196:199], v[48:51]\n\t"
    "ds_read_b64_tr_b16 a[168:169], v1 offset:8448\n\t"
    "v_cvt_pk_bf16_f32 v123, v16, v17\n\t"
    "v_mfma_f32_16x16x32_bf16 v[48:51], a[152:155], v[200:203], v[48:51]\n\t"
    "ds_read_b64_tr_b16 a[170:171], v1 offset:12544\n\t"
    "v_mfma_f32_16x16x32_bf16 v[48:51], a[156:159], v[204:207], v[48:51]\n\t"
    "v_exp_f32 v10, v76\n\t"
    "v_mfma_f32_16x16x32_bf16 v[168:171], v[176:179], v[120:123], v[152:155]\n\t"
    "v_exp_f32 v11, v77\n\t"
    "v_mfma_f32_16x16x32_bf16 v[36:39], a[128:131], v[208:211], v[132:135]\n\t"
    "ds_read_b64_tr_b16 a[172:173], v2 offset:8448\n\t"
    "v_cvt_pk_bf16_f32 v124, v10, v11\n\t"
    "v_mfma_f32_16x16x32_bf16 v[36:39], a[132:135], v[212:215], v[36:39]\n\t"
    "buffer_load_dwordx4 v8, s[44:47], s58 offen offset:2048 nt lds\n\t"
    "v_mfma_f32_16x16x32_bf16 v[36:39], a[136:139], v[216:219], v[36:39]\n\t"
    "v_exp_f32 v12, v78\n\t"
    "v_mfma_f32_16x16x32_bf16 v[36:39], a[140:143], v[220:223], v[36:39]\n\t"
    "v_exp_f32 v13, v79\n\t"
    "v_mfma_f32_16x16x32_bf16 v[52:55], a[144:147], v[208:211], v[132:135]\n\t"
    "ds_read_b64_tr_b16 a[174:175], v2 offset:12544\n\t"
    "v_cvt_pk_bf16_f32 v125, v12, v13\n\t"
    "v_mfma_f32_16x16x32_bf16 v[52:55], a[148:151], v[212:215], v[52:55]\n\t"
    "ds_read_b64_tr_b16 a[176:177], v1 offset:8704\n\t"
    "v_max3_f32 v9, v160, v164, v168\n\t"
    "v_mfma_f32_16x16x32_bf16 v[52:55], a[152:155], v[216:219], v[52:55]\n\t"
    "v_exp_f32 v14, v92\n\t"
    "v_mfma_f32_16x16x32_bf16 v[52:55], a[156:159], v[220:223], v[52:55]\n\t"
    "v_exp_f32 v15, v93\n\t"
    "v_mfma_f32_16x16x32_bf16 v[40:43], a[128:131], v[224:227], v[136:139]\n\t"
    "ds_read_b64_tr_b16 a[178:179], v1 offset:12800\n\t"
    "v_cvt_pk_bf16_f32 v126, v14, v15\n\t"
    "v_mfma_f32_16x16x32_bf16 v[40:43], a[132:135], v[228:231], v[40:43]\n\t"
    "v_exp_f32 v16, v94\n\t"
    "v_mfma_f32_16x16x32_bf16 v[40:43], a[136:139], v[232:235], v[40:43]\n\t"
    "v_exp_f32 v17, v95\n\t"
    "v_mfma_f32_16x16x32_bf16 v[40:43], a[140:143], v[236:239], v[40:43]\n\t"
    "ds_read_b64_tr_b16 a[180:181], v2 offset:8704\n\t"
    "v_cvt_pk_bf16_f32 v127, v16, v17\n\t"
    "v_mfma_f32_16x16x32_bf16 v[56:59], a[144:147], v[224:227], v[136:139]\n\t"
    "ds_read_b64_tr_b16 a[182:183], v2 offset:12800\n\t"
    "s_mov_b32 s60, s61\n\t"
    "v_mfma_f32_16x16x32_bf16 v[56:59], a[148:151], v[228:231], v[56:59]\n\t"
    "ds_read_b64_tr_b16 a[184:185], v1 offset:8960\n\t"
    "v_mfma_f32_16x16x32_bf16 v[172:175], v[176:179], v[124:127], v[156:159]\n\t"
    "ds_read_b64_tr_b16 a[186:187], v1 offset:13056\n\t"
    "v_mfma_f32_16x16x32_bf16 v[56:59], a[152:155], v[232:235], v[56:59]\n\t"
    "ds_read_b64_tr_b16 a[188:189], v2 offset:8960\n\t"
    "v_mfma_f32_16x16x32_bf16 v[56:59], a[156:159], v[236:239], v[56:59]\n\t"
    "ds_read_b64_tr_b16 a[190:191], v2 offset:13056\n\t"
    "v_mfma_f32_16x16x32_bf16 v[44:47], a[128:131], v[240:243], v[140:143]\n\t"
    "buffer_load_dwordx4 v190, s[44:47], s58 offen offset:3072 nt lds\n\t"
    "v_mfma_f32_16x16x32_bf16 v[44:47], a[132:135], v[244:247], v[44:47]\n\t"
    "v_add_u32 v1, s60, v4\n\t"
    "v_add_u32 v2, s60, v5\n\t"
    "v_mfma_f32_16x16x32_bf16 v[44:47], a[136:139], v[248:251], v[44:47]\n\t"
    "v_max_f32 v9, v9, v172\n\t"
    "v_cmp_le_f32 vcc, s63, v9\n\t"
    "v_mfma_f32_16x16x32_bf16 v[44:47], a[140:143], v[252:255], v[44:47]\n\t"
    "v_mfma_f32_16x16x32_bf16 v[60:63], a[144:147], v[240:243], v[140:143]\n\t"
    "s_cbranch_vccz L_ret1p7_%=\n\t"
    "s_swappc_b64 s[34:35], s[32:33]\n\t"
    "L_ret1p7_%=:\n\t"
    "v_mfma_f32_16x16x32_bf16 v[60:63], a[148:151], v[244:247], v[60:63]\n\t"
    "s_waitcnt lgkmcnt(0)\n\t"
    "v_mfma_f32_16x16x32_bf16 v[60:63], a[152:155], v[248:251], v[60:63]\n\t"
    "s_waitcnt vmcnt(20)\n\t"
    "s_barrier\n\t"
    "v_mfma_f32_16x16x32_bf16 v[60:63], a[156:159], v[252:255], v[60:63]\n\t"
    "s_sleep 1\n\t"
    "s_sub_u32 s62, s62, 1\n\t"
    ".p2align 6\n\t"
    "L_loop_%=:\n\t"
    "v_mfma_f32_16x16x32_bf16 a[0:3], a[160:163], v[112:115], a[0:3]\n\t"
    "v_exp_f32 v10, v32\n\t"
    "v_mfma_f32_16x16x32_bf16 a[16:19], a[164:167], v[112:115], a[16:19]\n\t"
    "v_exp_f32 v11, v33\n\t"
    "v_mfma_f32_16x16x32_bf16 a[32:35], a[168:171], v[112:115], a[32:35]\n\t"
    "ds_read_b128 a[128:131], v0 offset:8192\n\t"
    "v_cvt_pk_bf16_f32 v96, v10, v11\n\t"
    "v_mfma_f32_16x16x32_bf16 a[48:51], a[172:175], v[112:115], a[48:51]\n\t"
    "v_exp_f32 v12, v34\n\t"
    "v_mfma_f32_16x16x32_bf16 a[64:67], a[176:179], v[112:115], a[64:67]\n\t"
    "v_exp_f32 v13, v35\n\t"
    "v_mfma_f32_16x16x32_bf16 a[80:83], a[180:183], v[112:115], a[80:83]\n\t"
    "ds_read_b128 a[132:135], v0 offset:8448\n\t"
    "v_cvt_pk_bf16_f32 v97, v12, v13\n\t"
    "v_mfma_f32_16x16x32_bf16 a[96:99], a[184:187], v[112:115], a[96:99]\n\t"
    "s_add_u32 m0, s59, s56\n\t"
    "s_add_u32 s61, s61, 16384\n\t"
    "v_mfma_f32_16x16x32_bf16 a[112:115], a[188:191], v[112:115], a[112:115]\n\t"
    "v_exp_f32 v14, v48\n\t"
    "v_mfma_f32_16x16x32_bf16 a[4:7], a[160:163], v[116:119], a[4:7]\n\t"
    "v_exp_f32 v15, v49\n\t"
    "v_mfma_f32_16x16x32_bf16 a[20:23], a[164:167], v[116:119], a[20:23]\n\t"
    "ds_read_b128 a[136:139], v0 offset:8704\n\t"
    "v_cvt_pk_bf16_f32 v98, v14, v15\n\t"
    "v_mfma_f32_16x16x32_bf16 a[36:39], a[168:171], v[116:119], a[36:39]\n\t"
    "buffer_load_dwordx4 v6, s[40:43], s58 offen nt lds\n\t"
    "v_mfma_f32_16x16x32_bf16 a[52:55], a[172:175], v[116:119], a[52:55]\n\t"
    "v_exp_f32 v16, v50\n\t"
    "v_mfma_f32_16x16x32_bf16 a[68:71], a[176:179], v[116:119], a[68:71]\n\t"
    "v_exp_f32 v17, v51\n\t"
    "v_mfma_f32_16x16x32_bf16 a[84:87], a[180:183], v[116:119], a[84:87]\n\t"
    "ds_read_b128 a[140:143], v0 offset:8960\n\t"
    "v_cvt_pk_bf16_f32 v99, v16, v17\n\t"
    "v_mfma_f32_16x16x32_bf16 a[100:103], a[184:187], v[116:119], a[100:103]\n\t"
    "v_exp_f32 v10, v36\n\t"
    "v_mfma_f32_16x16x32_bf16 a[116:119], a[188:191], v[116:119], a[116:119]\n\t"
    "v_exp_f32 v11, v37\n\t"
    "v_mfma_f32_16x16x32_bf16 v[144:147], v[176:179], v[96:99], v[160:163]\n\t"
    "ds_read_b128 a[144:147], v0 offset:12288\n\t"
    "v_cvt_pk_bf16_f32 v100, v10, v11\n\t"
    "v_mfma_f32_16x16x32_bf16 a[8:11], a[160:163], v[120:123], a[8:11]\n\t"
    "v_exp_f32 v12, v38\n\t"
    "v_mfma_f32_16x16x32_bf16 a[24:27], a[164:167], v[120:123], a[24:27]\n\t"
    "v_exp_f32 v13, v39\n\t"
    "v_mfma_f32_16x16x32_bf16 a[40:43], a[168:171], v[120:123], a[40:43]\n\t"
    "ds_read_b128 a[148:151], v0 offset:12544\n\t"
    "v_cvt_pk_bf16_f32 v101, v12, v13\n\t"
    "v_mfma_f32_16x16x32_bf16 a[56:59], a[172:175], v[120:123], a[56:59]\n\t"
    "v_exp_f32 v14, v52\n\t"
    "v_mfma_f32_16x16x32_bf16 a[72:75], a[176:179], v[120:123], a[72:75]\n\t"
    "v_exp_f32 v15, v53\n\t"
    "v_mfma_f32_16x16x32_bf16 a[88:91], a[180:183], v[120:123], a[88:91]\n\t"
    "ds_read_b128 a[152:155], v0 offset:12800\n\t"
    "v_cvt_pk_bf16_f32 v102, v14, v15\n\t"
    "v_mfma_f32_16x16x32_bf16 a[104:107], a[184:187], v[120:123], a[104:107]\n\t"
    "v_exp_f32 v16, v54\n\t"
    "v_mfma_f32_16x16x32_bf16 a[120:123], a[188:191], v[120:123], a[120:123]\n\t"
    "v_exp_f32 v17, v55\n\t"
    "v_mfma_f32_16x16x32_bf16 a[12:15], a[160:163], v[124:127], a[12:15]\n\t"
    "ds_read_b128 a[156:159], v0 offset:13056\n\t"
    "v_cvt_pk_bf16_f32 v103, v16, v17\n\t"
    "v_mfma_f32_16x16x32_bf16 a[28:31], a[164:167], v[124:127], a[28:31]\n\t"
    "buffer_load_dwordx4 v7, s[40:43], s58 offen offset:1024 nt lds\n\t"
    "v_mfma_f32_16x16x32_bf16 a[44:47], a[168:171], v[124:127], a[44:47]\n\t"
    "v_exp_f32 v10, v40\n\t"
    "v_mfma_f32_16x16x32_bf16 v[148:151], v[176:179], v[100:103], v[164:167]\n\t"
    "v_exp_f32 v11, v41\n\t"
    "v_mfma_f32_16x16x32_bf16 a[60:63], a[172:175], v[124:127], a[60:63]\n\t"
    "ds_read_b64_tr_b16 a[160:161], v1 offset:0\n\t"
    "v_cvt_pk_bf16_f32 v104, v10, v11\n\t"
    "v_mfma_f32_16x16x32_bf16 a[76:79], a[176:179], v[124:127], a[76:79]\n\t"
    "v_exp_f32 v12, v42\n\t"
    "v_mfma_f32_16x16x32_bf16 a[92:95], a[180:183], v[124:127], a[92:95]\n\t"
    "v_exp_f32 v13, v43\n\t"
    "v_mfma_f32_16x16x32_bf16 a[108:111], a[184:187], v[124:127], a[108:111]\n\t"
    "ds_read_b64_tr_b16 a[162:163], v1 offset:4096\n\t"
    "v_cvt_pk_bf16_f32 v105, v12, v13\n\t"
    "v_mfma_f32_16x16x32_bf16 a[124:127], a[188:191], v[124:127], a[124:127]\n\t"
    "ds_read_b64_tr_b16 a[164:165], v2 offset:0\n\t"
    "s_waitcnt lgkmcnt(3)\n\t"
    "v_mfma_f32_16x16x32_bf16 v[64:67], a[128:131], v[192:195], v[128:131]\n\t"
    "v_exp_f32 v14, v56\n\t"
    "v_mfma_f32_16x16x32_bf16 v[64:67], a[132:135], v[196:199], v[64:67]\n\t"
    "v_exp_f32 v15, v57\n\t"
    "v_mfma_f32_16x16x32_bf16 v[64:67], a[136:139], v[200:203], v[64:67]\n\t"
    "ds_read_b64_tr_b16 a[166:167], v2 offset:4096\n\t"
    "v_cvt_pk_bf16_f32 v106, v14, v15\n\t"
    "v_mfma_f32_16x16x32_bf16 v[64:67], a[140:143], v[204:207], v[64:67]\n\t"
    "v_exp_f32 v16, v58\n\t"
    "v_mfma_f32_16x16x32_bf16 v[80:83], a[144:147], v[192:195], v[128:131]\n\t"
    "v_exp_f32 v17, v59\n\t"
    "v_mfma_f32_16x16x32_bf16 v[80:83], a[148:151], v[196:199], v[80:83]\n\t"
    "ds_read_b64_tr_b16 a[168:169], v1 offset:256\n\t"
    "v_cvt_pk_bf16_f32 v107, v16, v17\n\t"
    "v_mfma_f32_16x16x32_bf16 v[80:83], a[152:155], v[200:203], v[80:83]\n\t"
    "ds_read_b64_tr_b16 a[170:171], v1 offset:4352\n\t"
    "v_mfma_f32_16x16x32_bf16 v[80:83], a[156:159], v[204:207], v[80:83]\n\t"
    "v_exp_f32 v10, v44\n\t"
    "v_mfma_f32_16x16x32_bf16 v[152:155], v[176:179], v[104:107], v[168:171]\n\t"
    "v_exp_f32 v11, v45\n\t"
    "v_mfma_f32_16x16x32_bf16 v[68:71], a[128:131], v[208:211], v[132:135]\n\t"
    "ds_read_b64_tr_b16 a[172:173], v2 offset:256\n\t"
    "v_cvt_pk_bf16_f32 v108, v10, v11\n\t"
    "v_mfma_f32_16x16x32_bf16 v[68:71], a[132:135], v[212:215], v[68:71]\n\t"
    "buffer_load_dwordx4 v188, s[40:43], s58 offen offset:2048 nt lds\n\t"
    "v_mfma_f32_16x16x32_bf16 v[68:71], a[136:139], v[216:219], v[68:71]\n\t"
    "v_exp_f32 v12, v46\n\t"
    "v_mfma_f32_16x16x32_bf16 v[68:71], a[140:143], v[220:223], v[68:71]\n\t"
    "v_exp_f32 v13, v47\n\t"
    "v_mfma_f32_16x16x32_bf16 v[84:87], a[144:147], v[208:211], v[132:135]\n\t"
    "ds_read_b64_tr_b16 a[174:175], v2 offset:4352\n\t"
    "v_cvt_pk_bf16_f32 v109, v12, v13\n\t"
    "v_mfma_f32_16x16x32_bf16 v[84:87], a[148:151], v[212:215], v[84:87]\n\t"
    "ds_read_b64_tr_b16 a[176:177], v1 offset:512\n\t"
    "v_max3_f32 v9, v144, v148, v152\n\t"
    "v_mfma_f32_16x16x32_bf16 v[84:87], a[152:155], v[216:219], v[84:87]\n\t"
    "v_exp_f32 v14, v60\n\t"
    "v_mfma_f32_16x16x32_bf16 v[84:87], a[156:159], v[220:223], v[84:87]\n\t"
    "v_exp_f32 v15, v61\n\t"
    "v_mfma_f32_16x16x32_bf16 v[72:75], a[128:131], v[224:227], v[136:139]\n\t"
    "ds_read_b64_tr_b16 a[178:179], v1 offset:4608\n\t"
    "v_cvt_pk_bf16_f32 v110, v14, v15\n\t"
    "v_mfma_f32_16x16x32_bf16 v[72:75], a[132:135], v[228:231], v[72:75]\n\t"
    "v_exp_f32 v16, v62\n\t"
    "v_mfma_f32_16x16x32_bf16 v[72:75], a[136:139], v[232:235], v[72:75]\n\t"
    "v_exp_f32 v17, v63\n\t"
    "v_mfma_f32_16x16x32_bf16 v[72:75], a[140:143], v[236:239], v[72:75]\n\t"
    "ds_read_b64_tr_b16 a[180:181], v2 offset:512\n\t"
    "v_cvt_pk_bf16_f32 v111, v16, v17\n\t"
    "v_mfma_f32_16x16x32_bf16 v[88:91], a[144:147], v[224:227], v[136:139]\n\t"
    "ds_read_b64_tr_b16 a[182:183], v2 offset:4608\n\t"
    "s_cmp_eq_u32 s61, 81920\n\t"
    "v_mfma_f32_16x16x32_bf16 v[88:91], a[148:151], v[228:231], v[88:91]\n\t"
    "ds_read_b64_tr_b16 a[184:185], v1 offset:768\n\t"
    "s_cselect_b32 s61, 0, s61\n\t"
    "v_mfma_f32_16x16x32_bf16 v[156:159], v[176:179], v[108:111], v[172:175]\n\t"
    "ds_read_b64_tr_b16 a[186:187], v1 offset:4864\n\t"
    "v_add_u32 v0, s61, v3\n\t"
    "v_mfma_f32_16x16x32_bf16 v[88:91], a[152:155], v[232:235], v[88:91]\n\t"
    "ds_read_b64_tr_b16 a[188:189], v2 offset:768\n\t"
    "v_mfma_f32_16x16x32_bf16 v[88:91], a[156:159], v[236:239], v[88:91]\n\t"
    "ds_read_b64_tr_b16 a[190:191], v2 offset:4864\n\t"
    "v_mfma_f32_16x16x32_bf16 v[76:79], a[128:131], v[240:243], v[140:143]\n\t"
    "buffer_load_dwordx4 v189, s[40:43], s58 offen offset:3072 nt lds\n\t"
    "v_mfma_f32_16x16x32_bf16 v[76:79], a[132:135], v[244:247], v[76:79]\n\t"
    "s_add_u32 s58, s58, 16384\n\t"
    "s_memrealtime s[38:39]\n\t"
    "v_mfma_f32_16x16x32_bf16 v[76:79], a[136:139], v[248:251], v[76:79]\n\t"
    "v_max_f32 v9, v9, v156\n\t"
    "v_cmp_le_f32 vcc, s63, v9\n\t"
    "v_mfma_f32_16x16x32_bf16 v[76:79], a[140:143], v[252:255], v[76:79]\n\t"
    "v_mfma_f32_16x16x32_bf16 v[92:95], a[144:147], v[240:243], v[140:143]\n\t"
    "s_cbranch_vccnz L_rare0_%=\n\t"
    "L_ret0_%=:\n\t"
    "v_mfma_f32_16x16x32_bf16 v[92:95], a[148:151], v[244:247], v[92:95]\n\t"
    "s_waitcnt lgkmcnt(0)\n\t"
    "v_mfma_f32_16x16x32_bf16 v[92:95], a[152:155], v[248:251], v[92:95]\n\t"
    "s_sub_u32 s37, s36, s38\n\t"
    "s_cmp_gt_i32 s37, 0\n\t"
    "v_mfma_f32_16x16x32_bf16 v[92:95], a[156:159], v[252:255], v[92:95]\n\t"
    "s_cbranch_scc1 L_wait0_%=\n\t"
    "L_back0_%=:\n\t"
    "s_add_u32 s36, s38, s27\n\t"
    "v_mfma_f32_16x16x32_bf16 a[0:3], a[160:163], v[96:99], a[0:3]\n\t"
    "v_exp_f32 v10, v64\n\t"
    "v_mfma_f32_16x16x32_bf16 a[16:19], a[164:167], v[96:99], a[16:19]\n\t"
    "v_exp_f32 v11, v65\n\t"
    "v_mfma_f32_16x16x32_bf16 a[32:35], a[168:171], v[96:99], a[32:35]\n\t"
    "ds_read_b128 a[128:131], v0 offset:0\n\t"
    "v_cvt_pk_bf16_f32 v112, v10, v11\n\t"
    "v_mfma_f32_16x16x32_bf16 a[48:51], a[172:175], v[96:99], a[48:51]\n\t"
    "v_exp_f32 v12, v66\n\t"
    "v_mfma_f32_16x16x32_bf16 a[64:67], a[176:179], v[96:99], a[64:67]\n\t"
    "v_exp_f32 v13, v67\n\t"
    "v_mfma_f32_16x16x32_bf16 a[80:83], a[180:183], v[96:99], a[80:83]\n\t"
    "ds_read_b128 a[132:135], v0 offset:256\n\t"
    "v_cvt_pk_bf16_f32 v113, v12, v13\n\t"
    "v_mfma_f32_16x16x32_bf16 a[96:99], a[184:187], v[96:99], a[96:99]\n\t"
    "s_add_u32 m0, s59, s57\n\t"
    "s_mov_b32 s59, s60\n\t"
    "v_mfma_f32_16x16x32_bf16 a[112:115], a[188:191], v[96:99], a[112:115]\n\t"
    "v_exp_f32 v14, v80\n\t"
    "v_mfma_f32_16x16x32_bf16 a[4:7], a[160:163], v[100:103], a[4:7]\n\t"
    "v_exp_f32 v15, v81\n\t"
    "v_mfma_f32_16x16x32_bf16 a[20:23], a[164:167], v[100:103], a[20:23]\n\t"
    "ds_read_b128 a[136:139], v0 offset:512\n\t"
    "v_cvt_pk_bf16_f32 v114, v14, v15\n\t"
    "v_mfma_f32_16x16x32_bf16 a[36:39], a[168:171], v[100:103], a[36:39]\n\t"
    "buffer_load_dwordx4 v8, s[44:47], s58 offen nt lds\n\t"
    "v_mfma_f32_16x16x32_bf16 a[52:55], a[172:175], v[100:103], a[52:55]\n\t"
    "v_exp_f32 v16, v82\n\t"
    "v_mfma_f32_16x16x32_bf16 a[68:71], a[176:179], v[100:103], a[68:71]\n\t"
    "v_exp_f32 v17, v83\n\t"
    "v_mfma_f32_16x16x32_bf16 a[84:87], a[180:183], v[100:103], a[84:87]\n\t"
    "ds_read_b128 a[140:143], v0 offset:768\n\t"
    "v_cvt_pk_bf16_f32 v115, v16, v17\n\t"
    "v_mfma_f32_16x16x32_bf16 a[100:103], a[184:187], v[100:103], a[100:103]\n\t"
    "v_exp_f32 v10, v68\n\t"
    "v_mfma_f32_16x16x32_bf16 a[116:119], a[188:191], v[100:103], a[116:119]\n\t"
    "v_exp_f32 v11, v69\n\t"
    "v_mfma_f32_16x16x32_bf16 v[160:163], v[176:179], v[112:115], v[144:147]\n\t"
    "ds_read_b128 a[144:147], v0 offset:4096\n\t"
    "v_cvt_pk_bf16_f32 v116, v10, v11\n\t"
    "v_mfma_f32_16x16x32_bf16 a[8:11], a[160:163], v[104:107], a[8:11]\n\t"
    "v_exp_f32 v12, v70\n\t"
    "v_mfma_f32_16x16x32_bf16 a[24:27], a[164:167], v[104:107], a[24:27]\n\t"
    "v_exp_f32 v13, v71\n\t"
    "v_mfma_f32_16x16x32_bf16 a[40:43], a[168:171], v[104:107], a[40:43]\n\t"
    "ds_read_b128 a[148:151], v0 offset:4352\n\t"
    "v_cvt_pk_bf16_f32 v117, v12, v13\n\t"
    "v_mfma_f32_16x16x32_bf16 a[56:59], a[172:175], v[104:107], a[56:59]\n\t"
    "v_exp_f32 v14, v84\n\t"
    "v_mfma_f32_16x16x32_bf16 a[72:75], a[176:179], v[104:107], a[72:75]\n\t"
    "v_exp_f32 v15, v85\n\t"
    "v_mfma_f32_16x16x32_bf16 a[88:91], a[180:183], v[104:107], a[88:91]\n\t"
    "ds_read_b128 a[152:155], v0 offset:4608\n\t"
    "v_cvt_pk_bf16_f32 v118, v14, v15\n\t"
    "v_mfma_f32_16x16x32_bf16 a[104:107], a[184:187], v[104:107], a[104:107]\n\t"
    "v_exp_f32 v16, v86\n\t"
    "v_mfma_f32_16x16x32_bf16 a[120:123], a[188:191], v[104:107], a[120:123]\n\t"
    "v_exp_f32 v17, v87\n\t"
    "v_mfma_f32_16x16x32_bf16 a[12:15], a[160:163], v[108:111], a[12:15]\n\t"
    "ds_read_b128 a[156:159], v0 offset:4864\n\t"
    "v_cvt_pk_bf16_f32 v119, v16, v17\n\t"
    "v_mfma_f32_16x16x32_bf16 a[28:31], a[164:167], v[108:111], a[28:31]\n\t"
    "buffer_load_dwordx4 v190, s[44:47], s58 offen offset:1024 nt lds\n\t"
    "v_mfma_f32_16x16x32_bf16 a[44:47], a[168:171], v[108:111], a[44:47]\n\t"
    "v_exp_f32 v10, v72\n\t"
    "v_mfma_f32_16x16x32_bf16 v[164:167], v[176:179], v[116:119], v[148:151]\n\t"
    "v_exp_f32 v11, v73\n\t"
    "v_mfma_f32_16x16x32_bf16 a[60:63], a[172:175], v[108:111], a[60:63]\n\t"
    "ds_read_b64_tr_b16 a[160:161], v1 offset:8192\n\t"
    "v_cvt_pk_bf16_f32 v120, v10, v11\n\t"
    "v_mfma_f32_16x16x32_bf16 a[76:79], a[176:179], v[108:111], a[76:79]\n\t"
    "v_exp_f32 v12, v74\n\t"
    "v_mfma_f32_16x16x32_bf16 a[92:95], a[180:183], v[108:111], a[92:95]\n\t"
    "v_exp_f32 v13, v75\n\t"
    "v_mfma_f32_16x16x32_bf16 a[108:111], a[184:187], v[108:111], a[108:111]\n\t"
    "ds_read_b64_tr_b16 a[162:163], v1 offset:12288\n\t"
    "v_cvt_pk_bf16_f32 v121, v12, v13\n\t"
    "v_mfma_f32_16x16x32_bf16 a[124:127], a[188:191], v[108:111], a[124:127]\n\t"
    "ds_read_b64_tr_b16 a[164:165], v2 offset:8192\n\t"
    "s_waitcnt lgkmcnt(3)\n\t"
    "v_mfma_f32_16x16x32_bf16 v[32:35], a[128:131], v[192:195], v[128:131]\n\t"
    "v_exp_f32 v14, v88\n\t"
    "v_mfma_f32_16x16x32_bf16 v[32:35], a[132:135], v[196:199], v[32:35]\n\t"
    "v_exp_f32 v15, v89\n\t"
    "v_mfma_f32_16x16x32_bf16 v[32:35], a[136:139], v[200:203], v[32:35]\n\t"
    "ds_read_b64_tr_b16 a[166:167], v2 offset:12288\n\t"
    "v_cvt_pk_bf16_f32 v122, v14, v15\n\t"
    "v_mfma_f32_16x16x32_bf16 v[32:35], a[140:143], v[204:207], v[32:35]\n\t"
    "v_exp_f32 v16, v90\n\t"
    "v_mfma_f32_16x16x32_bf16 v[48:51], a[144:147], v[192:195], v[128:131]\n\t"
    "v_exp_f32 v17, v91\n\t"
    "v_mfma_f32_16x16x32_bf16 v[48:51], a[148:151], v[196:199], v[48:51]\n\t"
    "ds_read_b64_tr_b16 a[168:169], v1 offset:8448\n\t"
    "v_cvt_pk_bf16_f32 v123, v16, v17\n\t"
    "v_mfma_f32_16x16x32_bf16 v[48:51], a[152:155], v[200:203], v[48:51]\n\t"
    "ds_read_b64_tr_b16 a[170:171], v1 offset:12544\n\t"
    "v_mfma_f32_16x16x32_bf16 v[48:51], a[156:159], v[204:207], v[48:51]\n\t"
    "v_exp_f32 v10, v76\n\t"
    "v_mfma_f32_16x16x32_bf16 v[168:171], v[176:179], v[120:123], v[152:155]\n\t"
    "v_exp_f32 v11, v77\n\t"
    "v_mfma_f32_16x16x32_bf16 v[36:39], a[128:131], v[208:211], v[132:135]\n\t"
    "ds_read_b64_tr_b16 a[172:173], v2 offset:8448\n\t"
    "v_cvt_pk_bf16_f32 v124, v10, v11\n\t"
    "v_mfma_f32_16x16x32_bf16 v[36:39], a[132:135], v[212:215], v[36:39]\n\t"
    "buffer_load_dwordx4 v8, s[44:47], s58 offen offset:2048 nt lds\n\t"
    "v_mfma_f32_16x16x32_bf16 v[36:39], a[136:139], v[216:219], v[36:39]\n\t"
    "v_exp_f32 v12, v78\n\t"
    "v_mfma_f32_16x16x32_bf16 v[36:39], a[140:143], v[220:223], v[36:39]\n\t"
    "v_exp_f32 v13, v79\n\t"
    "v_mfma_f32_16x16x32_bf16 v[52:55], a[144:147], v[208:211], v[132:135]\n\t"
    "ds_read_b64_tr_b16 a[174:175], v2 offset:12544\n\t"
    "v_cvt_pk_bf16_f32 v125, v12, v13\n\t"
    "v_mfma_f32_16x16x32_bf16 v[52:55], a[148:151], v[212:215], v[52:55]\n\t"
    "ds_read_b64_tr_b16 a[176:177], v1 offset:8704\n\t"
    "v_max3_f32 v9, v160, v164, v168\n\t"
    "v_mfma_f32_16x16x32_bf16 v[52:55], a[152:155], v[216:219], v[52:55]\n\t"
    "v_exp_f32 v14, v92\n\t"
    "v_mfma_f32_16x16x32_bf16 v[52:55], a[156:159], v[220:223], v[52:55]\n\t"
    "v_exp_f32 v15, v93\n\t"
    "v_mfma_f32_16x16x32_bf16 v[40:43], a[128:131], v[224:227], v[136:139]\n\t"
    "ds_read_b64_tr_b16 a[178:179], v1 offset:12800\n\t"
    "v_cvt_pk_bf16_f32 v126, v14, v15\n\t"
    "v_mfma_f32_16x16x32_bf16 v[40:43], a[132:135], v[228:231], v[40:43]\n\t"
    "v_exp_f32 v16, v94\n\t"
    "v_mfma_f32_16x16x32_bf16 v[40:43], a[136:139], v[232:235], v[40:43]\n\t"
    "v_exp_f32 v17, v95\n\t"
    "v_mfma_f32_16x16x32_bf16 v[40:43], a[140:143], v[236:239], v[40:43]\n\t"
    "ds_read_b64_tr_b16 a[180:181], v2 offset:8704\n\t"
    "v_cvt_pk_bf16_f32 v127, v16, v17\n\t"
    "v_mfma_f32_16x16x32_bf16 v[56:59], a[144:147], v[224:227], v[136:139]\n\t"
    "ds_read_b64_tr_b16 a[182:183], v2 offset:12800\n\t"
    "s_mov_b32 s60, s61\n\t"
    "v_mfma_f32_16x16x32_bf16 v[56:59], a[148:151], v[228:231], v[56:59]\n\t"
    "ds_read_b64_tr_b16 a[184:185], v1 offset:8960\n\t"
    "v_mfma_f32_16x16x32_bf16 v[172:175], v[176:179], v[124:127], v[156:159]\n\t"
    "ds_read_b64_tr_b16 a[186:187], v1 offset:13056\n\t"
    "v_mfma_f32_16x16x32_bf16 v[56:59], a[152:155], v[232:235], v[56:59]\n\t"
    "ds_read_b64_tr_b16 a[188:189], v2 offset:8960\n\t"
    "v_mfma_f32_16x16x32_bf16 v[56:59], a[156:159], v[236:239], v[56:59]\n\t"
    "ds_read_b64_tr_b16 a[190:191], v2 offset:13056\n\t"
    "v_mfma_f32_16x16x32_bf16 v[44:47], a[128:131], v[240:243], v[140:143]\n\t"
    "buffer_load_dwordx4 v190, s[44:47], s58 offen offset:3072 nt lds\n\t"
    "v_mfma_f32_16x16x32_bf16 v[44:47], a[132:135], v[244:247], v[44:47]\n\t"
    "v_add_u32 v1, s60, v4\n\t"
    "v_add_u32 v2, s60, v5\n\t"
    "v_mfma_f32_16x16x32_bf16 v[44:47], a[136:139], v[248:251], v[44:47]\n\t"
    "v_max_f32 v9, v9, v172\n\t"
    "v_cmp_le_f32 vcc, s63, v9\n\t"
    "v_mfma_f32_16x16x32_bf16 v[44:47], a[140:143], v[252:255], v[44:47]\n\t"
    "s_cmp_gt_u32 s62, 2\n\t"
    "s_cselect_b32 s27, 130, 0\n\t"
    "v_mfma_f32_16x16x32_bf16 v[60:63], a[144:147], v[240:243], v[140:143]\n\t"
    "s_cbranch_vccnz L_rare1_%=\n\t"
    "L_ret1_%=:\n\t"
    "v_mfma_f32_16x16x32_bf16 v[60:63], a[148:151], v[244:247], v[60:63]\n\t"
    "s_waitcnt lgkmcnt(0)\n\t"
    "v_mfma_f32_16x16x32_bf16 v[60:63], a[152:155], v[248:251], v[60:63]\n\t"
    "s_waitcnt vmcnt(20)\n\t"
    "s_barrier\n\t"
    "v_mfma_f32_16x16x32_bf16 v[60:63], a[156:159], v[252:255], v[60:63]\n\t"
    "s_sub_u32 s62, s62, 1\n\t"
    "s_cbranch_scc0 L_loop_%=\n\t"
    "v_mfma_f32_16x16x32_bf16 a[0:3], a[160:163], v[112:115], a[0:3]\n\t"
    "v_exp_f32 v10, v32\n\t"
    "v_mfma_f32_16x16x32_bf16 a[16:19], a[164:167], v[112:115], a[16:19]\n\t"
    "v_exp_f32 v11, v33\n\t"
    "v_mfma_f32_16x16x32_bf16 a[32:35], a[168:171], v[112:115], a[32:35]\n\t"
    "ds_read_b128 a[128:131], v0 offset:8192\n\t"
    "v_cvt_pk_bf16_f32 v96, v10, v11\n\t"
    "v_mfma_f32_16x16x32_bf16 a[48:51], a[172:175], v[112:115], a[48:51]\n\t"
    "v_exp_f32 v12, v34\n\t"
    "v_mfma_f32_16x16x32_bf16 a[64:67], a[176:179], v[112:115], a[64:67]\n\t"
    "v_exp_f32 v13, v35\n\t"
    "v_mfma_f32_16x16x32_bf16 a[80:83], a[180:183], v[112:115], a[80:83]\n\t"
    "ds_read_b128 a[132:135], v0 offset:8448\n\t"
    "v_cvt_pk_bf16_f32 v97, v12, v13\n\t"
    "v_mfma_f32_16x16x32_bf16 a[96:99], a[184:187], v[112:115], a[96:99]\n\t"
    "s_add_u32 m0, s59, s56\n\t"
    "s_add_u32 s61, s61, 16384\n\t"
    "v_mfma_f32_16x16x32_bf16 a[112:115], a[188:191], v[112:115], a[112:115]\n\t"
    "v_exp_f32 v14, v48\n\t"
    "v_mfma_f32_16x16x32_bf16 a[4:7], a[160:163], v[116:119], a[4:7]\n\t"
    "v_exp_f32 v15, v49\n\t"
    "v_mfma_f32_16x16x32_bf16 a[20:23], a[164:167], v[116:119], a[20:23]\n\t"
    "ds_read_b128 a[136:139], v0 offset:8704\n\t"
    "v_cvt_pk_bf16_f32 v98, v14, v15\n\t"
    "v_mfma_f32_16x16x32_bf16 a[36:39], a[168:171], v[116:119], a[36:39]\n\t"
    "v_mfma_f32_16x16x32_bf16 a[52:55], a[172:175], v[116:119], a[52:55]\n\t"
    "v_exp_f32 v16, v50\n\t"
    "v_mfma_f32_16x16x32_bf16 a[68:71], a[176:179], v[116:119], a[68:71]\n\t"
    "v_exp_f32 v17, v51\n\t"
    "v_mfma_f32_16x16x32_bf16 a[84:87], a[180:183], v[116:119], a[84:87]\n\t"
    "ds_read_b128 a[140:143], v0 offset:8960\n\t"
    "v_cvt_pk_bf16_f32 v99, v16, v17\n\t"
    "v_mfma_f32_16x16x32_bf16 a[100:103], a[184:187], v[116:119], a[100:103]\n\t"
    "v_exp_f32 v10, v36\n\t"
    "v_mfma_f32_16x16x32_bf16 a[116:119], a[188:191], v[116:119], a[116:119]\n\t"
    "v_exp_f32 v11, v37\n\t"
    "v_mfma_f32_16x16x32_bf16 v[144:147], v[176:179], v[96:99], v[160:163]\n\t"
    "ds_read_b128 a[144:147], v0 offset:12288\n\t"
    "v_cvt_pk_bf16_f32 v100, v10, v11\n\t"
    "v_mfma_f32_16x16x32_bf16 a[8:11], a[160:163], v[120:123], a[8:11]\n\t"
    "v_exp_f32 v12, v38\n\t"
    "v_mfma_f32_16x16x32_bf16 a[24:27], a[164:167], v[120:123], a[24:27]\n\t"
    "v_exp_f32 v13, v39\n\t"
    "v_mfma_f32_16x16x32_bf16 a[40:43], a[168:171], v[120:123], a[40:43]\n\t"
    "ds_read_b128 a[148:151], v0 offset:12544\n\t"
    "v_cvt_pk_bf16_f32 v101, v12, v13\n\t"
    "v_mfma_f32_16x16x32_bf16 a[56:59], a[172:175], v[120:123], a[56:59]\n\t"
    "v_exp_f32 v14, v52\n\t"
    "v_mfma_f32_16x16x32_bf16 a[72:75], a[176:179], v[120:123], a[72:75]\n\t"
    "v_exp_f32 v15, v53\n\t"
    "v_mfma_f32_16x16x32_bf16 a[88:91], a[180:183], v[120:123], a[88:91]\n\t"
    "ds_read_b128 a[152:155], v0 offset:12800\n\t"
    "v_cvt_pk_bf16_f32 v102, v14, v15\n\t"
    "v_mfma_f32_16x16x32_bf16 a[104:107], a[184:187], v[120:123], a[104:107]\n\t"
    "v_exp_f32 v16, v54\n\t"
    "v_mfma_f32_16x16x32_bf16 a[120:123], a[188:191], v[120:123], a[120:123]\n\t"
    "v_exp_f32 v17, v55\n\t"
    "v_mfma_f32_16x16x32_bf16 a[12:15], a[160:163], v[124:127], a[12:15]\n\t"
    "ds_read_b128 a[156:159], v0 offset:13056\n\t"
    "v_cvt_pk_bf16_f32 v103, v16, v17\n\t"
    "v_mfma_f32_16x16x32_bf16 a[28:31], a[164:167], v[124:127], a[28:31]\n\t"
    "v_mfma_f32_16x16x32_bf16 a[44:47], a[168:171], v[124:127], a[44:47]\n\t"
    "v_exp_f32 v10, v40\n\t"
    "v_mfma_f32_16x16x32_bf16 v[148:151], v[176:179], v[100:103], v[164:167]\n\t"
    "v_exp_f32 v11, v41\n\t"
    "v_mfma_f32_16x16x32_bf16 a[60:63], a[172:175], v[124:127], a[60:63]\n\t"
    "ds_read_b64_tr_b16 a[160:161], v1 offset:0\n\t"
    "v_cvt_pk_bf16_f32 v104, v10, v11\n\t"
    "v_mfma_f32_16x16x32_bf16 a[76:79], a[176:179], v[124:127], a[76:79]\n\t"
    "v_exp_f32 v12, v42\n\t"
    "v_mfma_f32_16x16x32_bf16 a[92:95], a[180:183], v[124:127], a[92:95]\n\t"
    "v_exp_f32 v13, v43\n\t"
    "v_mfma_f32_16x16x32_bf16 a[108:111], a[184:187], v[124:127], a[108:111]\n\t"
    "ds_read_b64_tr_b16 a[162:163], v1 offset:4096\n\t"
    "v_cvt_pk_bf16_f32 v105, v12, v13\n\t"
    "v_mfma_f32_16x16x32_bf16 a[124:127], a[188:191], v[124:127], a[124:127]\n\t"
    "ds_read_b64_tr_b16 a[164:165], v2 offset:0\n\t"
    "s_waitcnt lgkmcnt(3)\n\t"
    "v_mfma_f32_16x16x32_bf16 v[64:67], a[128:131], v[192:195], v[128:131]\n\t"
    "v_exp_f32 v14, v56\n\t"
    "v_mfma_f32_16x16x32_bf16 v[64:67], a[132:135], v[196:199], v[64:67]\n\t"
    "v_exp_f32 v15, v57\n\t"
    "v_mfma_f32_16x16x32_bf16 v[64:67], a[136:139], v[200:203], v[64:67]\n\t"
    "ds_read_b64_tr_b16 a[166:167], v2 offset:4096\n\t"
    "v_cvt_pk_bf16_f32 v106, v14, v15\n\t"
    "v_mfma_f32_16x16x32_bf16 v[64:67], a[140:143], v[204:207], v[64:67]\n\t"
    "v_exp_f32 v16, v58\n\t"
    "v_mfma_f32_16x16x32_bf16 v[80:83], a[144:147], v[192:195], v[128:131]\n\t"
    "v_exp_f32 v17, v59\n\t"
    "v_mfma_f32_16x16x32_bf16 v[80:83], a[148:151], v[196:199], v[80:83]\n\t"
    "ds_read_b64_tr_b16 a[168:169], v1 offset:256\n\t"
    "v_cvt_pk_bf16_f32 v107, v16, v17\n\t"
    "v_mfma_f32_16x16x32_bf16 v[80:83], a[152:155], v[200:203], v[80:83]\n\t"
    "ds_read_b64_tr_b16 a[170:171], v1 offset:4352\n\t"
    "v_mfma_f32_16x16x32_bf16 v[80:83], a[156:159], v[204:207], v[80:83]\n\t"
    "v_exp_f32 v10, v44\n\t"
    "v_mfma_f32_16x16x32_bf16 v[152:155], v[176:179], v[104:107], v[168:171]\n\t"
    "v_exp_f32 v11, v45\n\t"
    "v_mfma_f32_16x16x32_bf16 v[68:71], a[128:131], v[208:211], v[132:135]\n\t"
    "ds_read_b64_tr_b16 a[172:173], v2 offset:256\n\t"
    "v_cvt_pk_bf16_f32 v108, v10, v11\n\t"
    "v_mfma_f32_16x16x32_bf16 v[68:71], a[132:135], v[212:215], v[68:71]\n\t"
    "v_mfma_f32_16x16x32_bf16 v[68:71], a[136:139], v[216:219], v[68:71]\n\t"
    "v_exp_f32 v12, v46\n\t"
    "v_mfma_f32_16x16x32_bf16 v[68:71], a[140:143], v[220:223], v[68:71]\n\t"
    "v_exp_f32 v13, v47\n\t"
    "v_mfma_f32_16x16x32_bf16 v[84:87], a[144:147], v[208:211], v[132:135]\n\t"
    "ds_read_b64_tr_b16 a[174:175], v2 offset:4352\n\t"
    "v_cvt_pk_bf16_f32 v109, v12, v13\n\t"
    "v_mfma_f32_16x16x32_bf16 v[84:87], a[148:151], v[212:215], v[84:87]\n\t"
    "ds_read_b64_tr_b16 a[176:177], v1 offset:512\n\t"
    "v_max3_f32 v9, v144, v148, v152\n\t"
    "v_mfma_f32_16x16x32_bf16 v[84:87], a[152:155], v[216:219], v[84:87]\n\t"
    "v_exp_f32 v14, v60\n\t"
    "v_mfma_f32_16x16x32_bf16 v[84:87], a[156:159], v[220:223], v[84:87]\n\t"
    "v_exp_f32 v15, v61\n\t"
    "v_mfma_f32_16x16x32_bf16 v[72:75], a[128:131], v[224:227], v[136:139]\n\t"
    "ds_read_b64_tr_b16 a[178:179], v1 offset:4608\n\t"
    "v_cvt_pk_bf16_f32 v110, v14, v15\n\t"
    "v_mfma_f32_16x16x32_bf16 v[72:75], a[132:135], v[228:231], v[72:75]\n\t"
    "v_exp_f32 v16, v62\n\t"
    "v_mfma_f32_16x16x32_bf16 v[72:75], a[136:139], v[232:235], v[72:75]\n\t"
    "v_exp_f32 v17, v63\n\t"
    "v_mfma_f32_16x16x32_bf16 v[72:75], a[140:143], v[236:239], v[72:75]\n\t"
    "ds_read_b64_tr_b16 a[180:181], v2 offset:512\n\t"
    "v_cvt_pk_bf16_f32 v111, v16, v17\n\t"
    "v_mfma_f32_16x16x32_bf16 v[88:91], a[144:147], v[224:227], v[136:139]\n\t"
    "ds_read_b64_tr_b16 a[182:183], v2 offset:4608\n\t"
    "s_cmp_eq_u32 s61, 81920\n\t"
    "v_mfma_f32_16x16x32_bf16 v[88:91], a[148:151], v[228:231], v[88:91]\n\t"
    "ds_read_b64_tr_b16 a[184:185], v1 offset:768\n\t"
    "s_cselect_b32 s61, 0, s61\n\t"
    "v_mfma_f32_16x16x32_bf16 v[156:159], v[176:179], v[108:111], v[172:175]\n\t"
    "ds_read_b64_tr_b16 a[186:187], v1 offset:4864\n\t"
    "v_add_u32 v0, s61, v3\n\t"
    "v_mfma_f32_16x16x32_bf16 v[88:91], a[152:155], v[232:235], v[88:91]\n\t"
    "ds_read_b64_tr_b16 a[188:189], v2 offset:768\n\t"
    "v_mfma_f32_16x16x32_bf16 v[88:91], a[156:159], v[236:239], v[88:91]\n\t"
    "ds_read_b64_tr_b16 a[190:191], v2 offset:4864\n\t"
    "v_mfma_f32_16x16x32_bf16 v[76:79], a[128:131], v[240:243], v[140:143]\n\t"
    "v_mfma_f32_16x16x32_bf16 v[76:79], a[132:135], v[244:247], v[76:79]\n\t"
    "s_add_u32 s58, s58, 16384\n\t"
    "v_mfma_f32_16x16x32_bf16 v[76:79], a[136:139], v[248:251], v[76:79]\n\t"
    "v_max_f32 v9, v9, v156\n\t"
    "v_cmp_le_f32 vcc, s63, v9\n\t"
    "v_mfma_f32_16x16x32_bf16 v[76:79], a[140:143], v[252:255], v[76:79]\n\t"
    "v_mfma_f32_16x16x32_bf16 v[92:95], a[144:147], v[240:243], v[140:143]\n\t"
    "s_cbranch_vccz L_ret0l0_%=\n\t"
    "s_swappc_b64 s[34:35], s[30:31]\n\t"
    "L_ret0l0_%=:\n\t"
    "v_mfma_f32_16x16x32_bf16 v[92:95], a[148:151], v[244:247], v[92:95]\n\t"
    "s_waitcnt lgkmcnt(0)\n\t"
    "v_mfma_f32_16x16x32_bf16 v[92:95], a[152:155], v[248:251], v[92:95]\n\t"
    "v_mfma_f32_16x16x32_bf16 v[92:95], a[156:159], v[252:255], v[92:95]\n\t"
    "v_mfma_f32_16x16x32_bf16 a[0:3], a[160:163], v[96:99], a[0:3]\n\t"
    "v_exp_f32 v10, v64\n\t"
    "v_mfma_f32_16x16x32_bf16 a[16:19], a[164:167], v[96:99], a[16:19]\n\t"
    "v_exp_f32 v11, v65\n\t"
    "v_mfma_f32_16x16x32_bf16 a[32:35], a[168:171], v[96:99], a[32:35]\n\t"
    "v_cvt_pk_bf16_f32 v112, v10, v11\n\t"
    "v_mfma_f32_16x16x32_bf16 a[48:51], a[172:175], v[96:99], a[48:51]\n\t"
    "v_exp_f32 v12, v66\n\t"
    "v_mfma_f32_16x16x32_bf16 a[64:67], a[176:179], v[96:99], a[64:67]\n\t"
    "v_exp_f32 v13, v67\n\t"
    "v_mfma_f32_16x16x32_bf16 a[80:83], a[180:183], v[96:99], a[80:83]\n\t"
    "v_cvt_pk_bf16_f32 v113, v12, v13\n\t"
    "v_mfma_f32_16x16x32_bf16 a[96:99], a[184:187], v[96:99], a[96:99]\n\t"
    "v_mfma_f32_16x16x32_bf16 a[112:115], a[188:191], v[96:99], a[112:115]\n\t"
    "v_exp_f32 v14, v80\n\t"
    "v_mfma_f32_16x16x32_bf16 a[4:7], a[160:163], v[100:103], a[4:7]\n\t"
    "v_exp_f32 v15, v81\n\t"
    "v_mfma_f32_16x16x32_bf16 a[20:23], a[164:167], v[100:103], a[20:23]\n\t"
    "v_cvt_pk_bf16_f32 v114, v14, v15\n\t"
    "v_mfma_f32_16x16x32_bf16 a[36:39], a[168:171], v[100:103], a[36:39]\n\t"
    "v_mfma_f32_16x16x32_bf16 a[52:55], a[172:175], v[100:103], a[52:55]\n\t"
    "v_exp_f32 v16, v82\n\t"
    "v_mfma_f32_16x16x32_bf16 a[68:71], a[176:179], v[100:103], a[68:71]\n\t"
    "v_exp_f32 v17, v83\n\t"
    "v_mfma_f32_16x16x32_bf16 a[84:87], a[180:183], v[100:103], a[84:87]\n\t"
    "v_cvt_pk_bf16_f32 v115, v16, v17\n\t"
    "v_mfma_f32_16x16x32_bf16 a[100:103], a[184:187], v[100:103], a[100:103]\n\t"
    "v_exp_f32 v10, v68\n\t"
    "v_mfma_f32_16x16x32_bf16 a[116:119], a[188:191], v[100:103], a[116:119]\n\t"
    "v_exp_f32 v11, v69\n\t"
    "s_nop 1\n\t"
    "v_mfma_f32_16x16x32_bf16 v[160:163], v[176:179], v[112:115], v[144:147]\n\t"
    "v_cvt_pk_bf16_f32 v116, v10, v11\n\t"
    "v_mfma_f32_16x16x32_bf16 a[8:11], a[160:163], v[104:107], a[8:11]\n\t"
    "v_exp_f32 v12, v70\n\t"
    "v_mfma_f32_16x16x32_bf16 a[24:27], a[164:167], v[104:107], a[24:27]\n\t"
    "v_exp_f32 v13, v71\n\t"
    "v_mfma_f32_16x16x32_bf16 a[40:43], a[168:171], v[104:107], a[40:43]\n\t"
    "v_cvt_pk_bf16_f32 v117, v12, v13\n\t"
    "v_mfma_f32_16x16x32_bf16 a[56:59], a[172:175], v[104:107], a[56:59]\n\t"
    "v_exp_f32 v14, v84\n\t"
    "v_mfma_f32_16x16x32_bf16 a[72:75], a[176:179], v[104:107], a[72:75]\n\t"
    "v_exp_f32 v15, v85\n\t"
    "v_mfma_f32_16x16x32_bf16 a[88:91], a[180:183], v[104:107], a[88:91]\n\t"
    "v_cvt_pk_bf16_f32 v118, v14, v15\n\t"
    "v_mfma_f32_16x16x32_bf16 a[104:107], a[184:187], v[104:107], a[104:107]\n\t"
    "v_exp_f32 v16, v86\n\t"
    "v_mfma_f32_16x16x32_bf16 a[120:123], a[188:191], v[104:107], a[120:123]\n\t"
    "v_exp_f32 v17, v87\n\t"
    "v_mfma_f32_16x16x32_bf16 a[12:15], a[160:163], v[108:111], a[12:15]\n\t"
    "v_cvt_pk_bf16_f32 v119, v16, v17\n\t"
    "v_mfma_f32_16x16x32_bf16 a[28:31], a[164:167], v[108:111], a[28:31]\n\t"
    "v_mfma_f32_16x16x32_bf16 a[44:47], a[168:171], v[108:111], a[44:47]\n\t"
    "v_exp_f32 v10, v72\n\t"
    "s_nop 1\n\t"
    "v_mfma_f32_16x16x32_bf16 v[164:167], v[176:179], v[116:119], v[148:151]\n\t"
    "v_exp_f32 v11, v73\n\t"
    "v_mfma_f32_16x16x32_bf16 a[60:63], a[172:175], v[108:111], a[60:63]\n\t"
    "ds_read_b64_tr_b16 a[160:161], v1 offset:8192\n\t"
    "v_cvt_pk_bf16_f32 v120, v10, v11\n\t"
    "v_mfma_f32_16x16x32_bf16 a[76:79], a[176:179], v[108:111], a[76:79]\n\t"
    "v_exp_f32 v12, v74\n\t"
    "v_mfma_f32_16x16x32_bf16 a[92:95], a[180:183], v[108:111], a[92:95]\n\t"
    "v_exp_f32 v13, v75\n\t"
    "v_mfma_f32_16x16x32_bf16 a[108:111], a[184:187], v[108:111], a[108:111]\n\t"
    "ds_read_b64_tr_b16 a[162:163], v1 offset:12288\n\t"
    "v_cvt_pk_bf16_f32 v121, v12, v13\n\t"
    "v_mfma_f32_16x16x32_bf16 a[124:127], a[188:191], v[108:111], a[124:127]\n\t"
    "ds_read_b64_tr_b16 a[164:165], v2 offset:8192\n\t"
    "v_exp_f32 v14, v88\n\t"
    "v_exp_f32 v15, v89\n\t"
    "ds_read_b64_tr_b16 a[166:167], v2 offset:12288\n\t"
    "v_cvt_pk_bf16_f32 v122, v14, v15\n\t"
    "v_exp_f32 v16, v90\n\t"
    "v_exp_f32 v17, v91\n\t"
    "ds_read_b64_tr_b16 a[168:169], v1 offset:8448\n\t"
    "v_cvt_pk_bf16_f32 v123, v16, v17\n\t"
    "ds_read_b64_tr_b16 a[170:171], v1 offset:12544\n\t"
    "v_exp_f32 v10, v76\n\t"
    "s_nop 1\n\t"
    "v_mfma_f32_16x16x32_bf16 v[168:171], v[176:179], v[120:123], v[152:155]\n\t"
    "v_exp_f32 v11, v77\n\t"
    "ds_read_b64_tr_b16 a[172:173], v2 offset:8448\n\t"
    "v_cvt_pk_bf16_f32 v124, v10, v11\n\t"
    "v_exp_f32 v12, v78\n\t"
    "v_exp_f32 v13, v79\n\t"
    "ds_read_b64_tr_b16 a[174:175], v2 offset:12544\n\t"
    "v_cvt_pk_bf16_f32 v125, v12, v13\n\t"
    "ds_read_b64_tr_b16 a[176:177], v1 offset:8704\n\t"
    "s_nop 7\n\t"
    "v_max3_f32 v9, v160, v164, v168\n\t"
    "v_exp_f32 v14, v92\n\t"
    "v_exp_f32 v15, v93\n\t"
    "ds_read_b64_tr_b16 a[178:179], v1 offset:12800\n\t"
    "v_cvt_pk_bf16_f32 v126, v14, v15\n\t"
    "v_exp_f32 v16, v94\n\t"
    "v_exp_f32 v17, v95\n\t"
    "ds_read_b64_tr_b16 a[180:181], v2 offset:8704\n\t"
    "v_cvt_pk_bf16_f32 v127, v16, v17\n\t"
    "ds_read_b64_tr_b16 a[182:183], v2 offset:12800\n\t"
    "ds_read_b64_tr_b16 a[184:185], v1 offset:8960\n\t"
    "s_nop 1\n\t"
    "v_mfma_f32_16x16x32_bf16 v[172:175], v[176:179], v[124:127], v[156:159]\n\t"
    "ds_read_b64_tr_b16 a[186:187], v1 offset:13056\n\t"
    "ds_read_b64_tr_b16 a[188:189], v2 offset:8960\n\t"
    "ds_read_b64_tr_b16 a[190:191], v2 offset:13056\n\t"
    "s_nop 7\n\t"
    "v_max_f32 v9, v9, v172\n\t"
    "v_cmp_le_f32 vcc, s63, v9\n\t"
    "s_cbranch_vccz L_ret1l1_%=\n\t"
    "s_swappc_b64 s[34:35], s[32:33]\n\t"
    "L_ret1l1_%=:\n\t"
    "s_waitcnt lgkmcnt(0)\n\t"
    "s_barrier\n\t"
    "v_mfma_f32_16x16x32_bf16 a[0:3], a[160:163], v[112:115], a[0:3]\n\t"
    "v_mbcnt_lo_u32_b32 v18, -1, 0\n\t"
    "v_mbcnt_hi_u32_b32 v18, -1, v18\n\t"
    "v_rcp_f32 v184, v160\n\t"
    "v_mfma_f32_16x16x32_bf16 a[16:19], a[164:167], v[112:115], a[16:19]\n\t"
    "v_rcp_f32 v186, v164\n\t"
    "v_rcp_f32 v188, v168\n\t"
    "v_rcp_f32 v30, v172\n\t"
    "v_mfma_f32_16x16x32_bf16 a[32:35], a[168:171], v[112:115], a[32:35]\n\t"
    "s_lshl_b32 s64, s68, 14\n\t"
    "s_lshl_b32 s65, s68, 12\n\t"
    "s_add_u32 s65, s65, s69\n\t"
    "v_mfma_f32_16x16x32_bf16 a[48:51], a[172:175], v[112:115], a[48:51]\n\t"
    "v_and_b32 v19, 15, v18\n\t"
    "v_lshrrev_b32 v20, 4, v18\n\t"
    "v_lshlrev_b32 v21, 8, v19\n\t"
    "v_mfma_f32_16x16x32_bf16 a[64:67], a[176:179], v[112:115], a[64:67]\n\t"
    "v_lshrrev_b32 v24, 1, v20\n\t"
    "v_xor_b32 v24, v24, v19\n\t"
    "v_lshl_add_u32 v21, v24, 4, v21\n\t"
    "v_mfma_f32_16x16x32_bf16 a[80:83], a[180:183], v[112:115], a[80:83]\n\t"
    "v_and_b32 v24, 1, v20\n\t"
    "v_lshl_add_u32 v21, v24, 3, v21\n\t"
    "v_add_u32 v21, s65, v21\n\t"
    "v_mfma_f32_16x16x32_bf16 a[96:99], a[184:187], v[112:115], a[96:99]\n\t"
    "v_xor_b32 v19, v19, v20\n\t"
    "v_lshlrev_b32 v25, 8, v20\n\t"
    "v_lshl_add_u32 v25, v19, 4, v25\n\t"
    "v_mfma_f32_16x16x32_bf16 a[112:115], a[188:191], v[112:115], a[112:115]\n\t"
    "v_add_u32 v25, s65, v25\n\t"
    "v_xor_b32 v26, 64, v25\n\t"
    "v_xor_b32 v27, 128, v25\n\t"
    "v_mfma_f32_16x16x32_bf16 a[4:7], a[160:163], v[116:119], a[4:7]\n\t"
    "v_xor_b32 v28, 192, v25\n\t"
    "v_lshlrev_b32 v29, 4, v18\n\t"
    "v_xor_b32 v64, 0, v21\n\t"
    "v_mfma_f32_16x16x32_bf16 a[20:23], a[164:167], v[116:119], a[20:23]\n\t"
    "v_xor_b32 v65, 32, v21\n\t"
    "v_xor_b32 v66, 64, v21\n\t"
    "v_xor_b32 v67, 96, v21\n\t"
    "v_mfma_f32_16x16x32_bf16 a[36:39], a[168:171], v[116:119], a[36:39]\n\t"
    "v_xor_b32 v68, 128, v21\n\t"
    "v_xor_b32 v69, 160, v21\n\t"
    "v_xor_b32 v70, 192, v21\n\t"
    "v_mfma_f32_16x16x32_bf16 a[52:55], a[172:175], v[116:119], a[52:55]\n\t"
    "v_xor_b32 v71, 224, v21\n\t"
    "v_mfma_f32_16x16x32_bf16 a[68:71], a[176:179], v[116:119], a[68:71]\n\t"
    "v_mfma_f32_16x16x32_bf16 a[84:87], a[180:183], v[116:119], a[84:87]\n\t"
    "v_mfma_f32_16x16x32_bf16 a[100:103], a[184:187], v[116:119], a[100:103]\n\t"
    "v_mfma_f32_16x16x32_bf16 a[116:119], a[188:191], v[116:119], a[116:119]\n\t"
    "s_waitcnt vmcnt(0)\n\t"
    "v_mfma_f32_16x16x32_bf16 a[8:11], a[160:163], v[120:123], a[8:11]\n\t"
    "v_accvgpr_read_b32 v32, a0\n\t"
    "v_accvgpr_read_b32 v33, a1\n\t"
    "v_accvgpr_read_b32 v34, a2\n\t"
    "v_mfma_f32_16x16x32_bf16 a[24:27], a[164:167], v[120:123], a[24:27]\n\t"
    "v_accvgpr_read_b32 v35, a3\n\t"
    "v_pk_mul_f32 v[32:33], v[32:33], v[184:185] op_sel_hi:[1,0]\n\t"
    "v_pk_mul_f32 v[34:35], v[34:35], v[184:185] op_sel_hi:[1,0]\n\t"
    "v_mfma_f32_16x16x32_bf16 a[40:43], a[168:171], v[120:123], a[40:43]\n\t"
    "v_cvt_pk_bf16_f32 v36, v32, v33\n\t"
    "v_cvt_pk_bf16_f32 v37, v34, v35\n\t"
    "s_nop 0\n\t"
    "v_mfma_f32_16x16x32_bf16 a[56:59], a[172:175], v[120:123], a[56:59]\n\t"
    "ds_write_b64 v64, v[36:37] offset:0\n\t"
    "v_accvgpr_read_b32 v40, a16\n\t"
    "v_accvgpr_read_b32 v41, a17\n\t"
    "v_mfma_f32_16x16x32_bf16 a[72:75], a[176:179], v[120:123], a[72:75]\n\t"
    "v_accvgpr_read_b32 v42, a18\n\t"
    "v_accvgpr_read_b32 v43, a19\n\t"
    "v_pk_mul_f32 v[40:41], v[40:41], v[184:185] op_sel_hi:[1,0]\n\t"
    "v_mfma_f32_16x16x32_bf16 a[88:91], a[180:183], v[120:123], a[88:91]\n\t"
    "v_pk_mul_f32 v[42:43], v[42:43], v[184:185] op_sel_hi:[1,0]\n\t"
    "v_cvt_pk_bf16_f32 v44, v40, v41\n\t"
    "v_cvt_pk_bf16_f32 v45, v42, v43\n\t"
    "v_mfma_f32_16x16x32_bf16 a[104:107], a[184:187], v[120:123], a[104:107]\n\t"
    "s_nop 0\n\t"
    "ds_write_b64 v65, v[44:45] offset:0\n\t"
    "v_accvgpr_read_b32 v48, a32\n\t"
    "v_mfma_f32_16x16x32_bf16 a[120:123], a[188:191], v[120:123], a[120:123]\n\t"
    "v_accvgpr_read_b32 v49, a33\n\t"
    "v_accvgpr_read_b32 v50, a34\n\t"
    "v_accvgpr_read_b32 v51, a35\n\t"
    "v_mfma_f32_16x16x32_bf16 a[12:15], a[160:163], v[124:127], a[12:15]\n\t"
    "v_pk_mul_f32 v[48:49], v[48:49], v[184:185] op_sel_hi:[1,0]\n\t"
    "v_pk_mul_f32 v[50:51], v[50:51], v[184:185] op_sel_hi:[1,0]\n\t"
    "v_cvt_pk_bf16_f32 v52, v48, v49\n\t"
    "v_mfma_f32_16x16x32_bf16 a[28:31], a[164:167], v[124:127], a[28:31]\n\t"
    "v_cvt_pk_bf16_f32 v53, v50, v51\n\t"
    "s_nop 0\n\t"
    "ds_write_b64 v66, v[52:53] offset:0\n\t"
    "v_mfma_f32_16x16x32_bf16 a[44:47], a[168:171], v[124:127], a[44:47]\n\t"
    "v_accvgpr_read_b32 v56, a48\n\t"
    "v_accvgpr_read_b32 v57, a49\n\t"
    "v_accvgpr_read_b32 v58, a50\n\t"
    "v_mfma_f32_16x16x32_bf16 a[60:63], a[172:175], v[124:127], a[60:63]\n\t"
    "v_accvgpr_read_b32 v59, a51\n\t"
    "v_pk_mul_f32 v[56:57], v[56:57], v[184:185] op_sel_hi:[1,0]\n\t"
    "v_pk_mul_f32 v[58:59], v[58:59], v[184:185] op_sel_hi:[1,0]\n\t"
    "v_mfma_f32_16x16x32_bf16 a[76:79], a[176:179], v[124:127], a[76:79]\n\t"
    "v_cvt_pk_bf16_f32 v60, v56, v57\n\t"
    "v_cvt_pk_bf16_f32 v61, v58, v59\n\t"
    "s_nop 0\n\t"
    "v_mfma_f32_16x16x32_bf16 a[92:95], a[180:183], v[124:127], a[92:95]\n\t"
    "ds_write_b64 v67, v[60:61] offset:0\n\t"
    "v_accvgpr_read_b32 v32, a64\n\t"
    "v_accvgpr_read_b32 v33, a65\n\t"
    "v_mfma_f32_16x16x32_bf16 a[108:111], a[184:187], v[124:127], a[108:111]\n\t"
    "v_accvgpr_read_b32 v34, a66\n\t"
    "v_accvgpr_read_b32 v35, a67\n\t"
    "v_pk_mul_f32 v[32:33], v[32:33], v[184:185] op_sel_hi:[1,0]\n\t"
    "v_mfma_f32_16x16x32_bf16 a[124:127], a[188:191], v[124:127], a[124:127]\n\t"
    "v_pk_mul_f32 v[34:35], v[34:35], v[184:185] op_sel_hi:[1,0]\n\t"
    "v_cvt_pk_bf16_f32 v36, v32, v33\n\t"
    "v_cvt_pk_bf16_f32 v37, v34, v35\n\t"
    "s_nop 0\n\t"
    "ds_write_b64 v68, v[36:37] offset:0\n\t"
    "v_accvgpr_read_b32 v40, a80\n\t"
    "v_accvgpr_read_b32 v41, a81\n\t"
    "v_accvgpr_read_b32 v42, a82\n\t"
    "v_accvgpr_read_b32 v43, a83\n\t"
    "v_pk_mul_f32 v[40:41], v[40:41], v[184:185] op_sel_hi:[1,0]\n\t"
    "v_pk_mul_f32 v[42:43], v[42:43], v[184:185] op_sel_hi:[1,0]\n\t"
    "v_cvt_pk_bf16_f32 v44, v40, v41\n\t"
    "v_cvt_pk_bf16_f32 v45, v42, v43\n\t"
    "s_nop 0\n\t"
    "ds_write_b64 v69, v[44:45] offset:0\n\t"
    "v_accvgpr_read_b32 v48, a96\n\t"
    "v_accvgpr_read_b32 v49, a97\n\t"
    "v_accvgpr_read_b32 v50, a98\n\t"
    "v_accvgpr_read_b32 v51, a99\n\t"
    "v_pk_mul_f32 v[48:49], v[48:49], v[184:185] op_sel_hi:[1,0]\n\t"
    "v_pk_mul_f32 v[50:51], v[50:51], v[184:185] op_sel_hi:[1,0]\n\t"
    "v_cvt_pk_bf16_f32 v52, v48, v49\n\t"
    "v_cvt_pk_bf16_f32 v53, v50, v51\n\t"
    "s_nop 0\n\t"
    "ds_write_b64 v70, v[52:53] offset:0\n\t"
    "v_accvgpr_read_b32 v56, a112\n\t"
    "v_accvgpr_read_b32 v57, a113\n\t"
    "v_accvgpr_read_b32 v58, a114\n\t"
    "v_accvgpr_read_b32 v59, a115\n\t"
    "v_pk_mul_f32 v[56:57], v[56:57], v[184:185] op_sel_hi:[1,0]\n\t"
    "v_pk_mul_f32 v[58:59], v[58:59], v[184:185] op_sel_hi:[1,0]\n\t"
    "v_cvt_pk_bf16_f32 v60, v56, v57\n\t"
    "v_cvt_pk_bf16_f32 v61, v58, v59\n\t"
    "s_nop 0\n\t"
    "ds_write_b64 v71, v[60:61] offset:0\n\t"
    "s_waitcnt lgkmcnt(0)\n\t"
    "ds_read_b128 v[96:99], v25 offset:0\n\t"
    "ds_read_b128 v[100:103], v26 offset:1024\n\t"
    "ds_read_b128 v[104:107], v27 offset:2048\n\t"
    "ds_read_b128 v[108:111], v28 offset:3072\n\t"
    "v_accvgpr_read_b32 v32, a4\n\t"
    "v_accvgpr_read_b32 v33, a5\n\t"
    "v_accvgpr_read_b32 v34, a6\n\t"
    "v_accvgpr_read_b32 v35, a7\n\t"
    "v_pk_mul_f32 v[32:33], v[32:33], v[186:187] op_sel_hi:[1,0]\n\t"
    "v_pk_mul_f32 v[34:35], v[34:35], v[186:187] op_sel_hi:[1,0]\n\t"
    "v_cvt_pk_bf16_f32 v36, v32, v33\n\t"
    "v_cvt_pk_bf16_f32 v37, v34, v35\n\t"
    "s_nop 0\n\t"
    "ds_write_b64 v64, v[36:37] offset:16384\n\t"
    "v_accvgpr_read_b32 v40, a20\n\t"
    "v_accvgpr_read_b32 v41, a21\n\t"
    "v_accvgpr_read_b32 v42, a22\n\t"
    "v_accvgpr_read_b32 v43, a23\n\t"
    "v_pk_mul_f32 v[40:41], v[40:41], v[186:187] op_sel_hi:[1,0]\n\t"
    "v_pk_mul_f32 v[42:43], v[42:43], v[186:187] op_sel_hi:[1,0]\n\t"
    "v_cvt_pk_bf16_f32 v44, v40, v41\n\t"
    "v_cvt_pk_bf16_f32 v45, v42, v43\n\t"
    "s_nop 0\n\t"
    "ds_write_b64 v65, v[44:45] offset:16384\n\t"
    "s_waitcnt lgkmcnt(2)\n\t"
    "s_add_u32 s66, s64, 0\n\t"
    "buffer_store_dwordx4 v[96:99], v29, s[52:55], s66 offen sc0 sc1\n\t"
    "buffer_store_dwordx4 v[100:103], v29, s[52:55], s66 offen offset:1024 sc0 sc1\n\t"
    "buffer_store_dwordx4 v[104:107], v29, s[52:55], s66 offen offset:2048 sc0 sc1\n\t"
    "buffer_store_dwordx4 v[108:111], v29, s[52:55], s66 offen offset:3072 sc0 sc1\n\t"
    "v_accvgpr_read_b32 v48, a36\n\t"
    "v_accvgpr_read_b32 v49, a37\n\t"
    "v_accvgpr_read_b32 v50, a38\n\t"
    "v_accvgpr_read_b32 v51, a39\n\t"
    "v_pk_mul_f32 v[48:49], v[48:49], v[186:187] op_sel_hi:[1,0]\n\t"
    "v_pk_mul_f32 v[50:51], v[50:51], v[186:187] op_sel_hi:[1,0]\n\t"
    "v_cvt_pk_bf16_f32 v52, v48, v49\n\t"
    "v_cvt_pk_bf16_f32 v53, v50, v51\n\t"
    "s_nop 0\n\t"
    "ds_write_b64 v66, v[52:53] offset:16384\n\t"
    "v_accvgpr_read_b32 v56, a52\n\t"
    "v_accvgpr_read_b32 v57, a53\n\t"
    "v_accvgpr_read_b32 v58, a54\n\t"
    "v_accvgpr_read_b32 v59, a55\n\t"
    "v_pk_mul_f32 v[56:57], v[56:57], v[186:187] op_sel_hi:[1,0]\n\t"
    "v_pk_mul_f32 v[58:59], v[58:59], v[186:187] op_sel_hi:[1,0]\n\t"
    "v_cvt_pk_bf16_f32 v60, v56, v57\n\t"
    "v_cvt_pk_bf16_f32 v61, v58, v59\n\t"
    "s_nop 0\n\t"
    "ds_write_b64 v67, v[60:61] offset:16384\n\t"
    "v_accvgpr_read_b32 v32, a68\n\t"
    "v_accvgpr_read_b32 v33, a69\n\t"
    "v_accvgpr_read_b32 v34, a70\n\t"
    "v_accvgpr_read_b32 v35, a71\n\t"
    "v_pk_mul_f32 v[32:33], v[32:33], v[186:187] op_sel_hi:[1,0]\n\t"
    "v_pk_mul_f32 v[34:35], v[34:35], v[186:187] op_sel_hi:[1,0]\n\t"
    "v_cvt_pk_bf16_f32 v36, v32, v33\n\t"
    "v_cvt_pk_bf16_f32 v37, v34, v35\n\t"
    "s_nop 0\n\t"
    "ds_write_b64 v68, v[36:37] offset:16384\n\t"
    "v_accvgpr_read_b32 v40, a84\n\t"
    "v_accvgpr_read_b32 v41, a85\n\t"
    "v_accvgpr_read_b32 v42, a86\n\t"
    "v_accvgpr_read_b32 v43, a87\n\t"
    "v_pk_mul_f32 v[40:41], v[40:41], v[186:187] op_sel_hi:[1,0]\n\t"
    "v_pk_mul_f32 v[42:43], v[42:43], v[186:187] op_sel_hi:[1,0]\n\t"
    "v_cvt_pk_bf16_f32 v44, v40, v41\n\t"
    "v_cvt_pk_bf16_f32 v45, v42, v43\n\t"
    "s_nop 0\n\t"
    "ds_write_b64 v69, v[44:45] offset:16384\n\t"
    "v_accvgpr_read_b32 v48, a100\n\t"
    "v_accvgpr_read_b32 v49, a101\n\t"
    "v_accvgpr_read_b32 v50, a102\n\t"
    "v_accvgpr_read_b32 v51, a103\n\t"
    "v_pk_mul_f32 v[48:49], v[48:49], v[186:187] op_sel_hi:[1,0]\n\t"
    "v_pk_mul_f32 v[50:51], v[50:51], v[186:187] op_sel_hi:[1,0]\n\t"
    "v_cvt_pk_bf16_f32 v52, v48, v49\n\t"
    "v_cvt_pk_bf16_f32 v53, v50, v51\n\t"
    "s_nop 0\n\t"
    "ds_write_b64 v70, v[52:53] offset:16384\n\t"
    "v_accvgpr_read_b32 v56, a116\n\t"
    "v_accvgpr_read_b32 v57, a117\n\t"
    "v_accvgpr_read_b32 v58, a118\n\t"
    "v_accvgpr_read_b32 v59, a119\n\t"
    "v_pk_mul_f32 v[56:57], v[56:57], v[186:187] op_sel_hi:[1,0]\n\t"
    "v_pk_mul_f32 v[58:59], v[58:59], v[186:187] op_sel_hi:[1,0]\n\t"
    "v_cvt_pk_bf16_f32 v60, v56, v57\n\t"
    "v_cvt_pk_bf16_f32 v61, v58, v59\n\t"
    "s_nop 0\n\t"
    "ds_write_b64 v71, v[60:61] offset:16384\n\t"
    "s_waitcnt lgkmcnt(0)\n\t"
    "ds_read_b128 v[112:115], v25 offset:16384\n\t"
    "ds_read_b128 v[116:119], v26 offset:17408\n\t"
    "ds_read_b128 v[120:123], v27 offset:18432\n\t"
    "ds_read_b128 v[124:127], v28 offset:19456\n\t"
    "v_accvgpr_read_b32 v32, a8\n\t"
    "v_accvgpr_read_b32 v33, a9\n\t"
    "v_accvgpr_read_b32 v34, a10\n\t"
    "v_accvgpr_read_b32 v35, a11\n\t"
    "v_pk_mul_f32 v[32:33], v[32:33], v[188:189] op_sel_hi:[1,0]\n\t"
    "v_pk_mul_f32 v[34:35], v[34:35], v[188:189] op_sel_hi:[1,0]\n\t"
    "v_cvt_pk_bf16_f32 v36, v32, v33\n\t"
    "v_cvt_pk_bf16_f32 v37, v34, v35\n\t"
    "s_nop 0\n\t"
    "ds_write_b64 v64, v[36:37] offset:32768\n\t"
    "v_accvgpr_read_b32 v40, a24\n\t"
    "v_accvgpr_read_b32 v41, a25\n\t"
    "v_accvgpr_read_b32 v42, a26\n\t"
    "v_accvgpr_read_b32 v43, a27\n\t"
    "v_pk_mul_f32 v[40:41], v[40:41], v[188:189] op_sel_hi:[1,0]\n\t"
    "v_pk_mul_f32 v[42:43], v[42:43], v[188:189] op_sel_hi:[1,0]\n\t"
    "v_cvt_pk_bf16_f32 v44, v40, v41\n\t"
    "v_cvt_pk_bf16_f32 v45, v42, v43\n\t"
    "s_nop 0\n\t"
    "ds_write_b64 v65, v[44:45] offset:32768\n\t"
    "s_waitcnt lgkmcnt(2)\n\t"
    "s_add_u32 s66, s64, 4096\n\t"
    "buffer_store_dwordx4 v[112:115], v29, s[52:55], s66 offen sc0 sc1\n\t"
    "buffer_store_dwordx4 v[116:119], v29, s[52:55], s66 offen offset:1024 sc0 sc1\n\t"
    "buffer_store_dwordx4 v[120:123], v29, s[52:55], s66 offen offset:2048 sc0 sc1\n\t"
    "buffer_store_dwordx4 v[124:127], v29, s[52:55], s66 offen offset:3072 sc0 sc1\n\t"
    "v_accvgpr_read_b32 v48, a40\n\t"
    "v_accvgpr_read_b32 v49, a41\n\t"
    "v_accvgpr_read_b32 v50, a42\n\t"
    "v_accvgpr_read_b32 v51, a43\n\t"
    "v_pk_mul_f32 v[48:49], v[48:49], v[188:189] op_sel_hi:[1,0]\n\t"
    "v_pk_mul_f32 v[50:51], v[50:51], v[188:189] op_sel_hi:[1,0]\n\t"
    "v_cvt_pk_bf16_f32 v52, v48, v49\n\t"
    "v_cvt_pk_bf16_f32 v53, v50, v51\n\t"
    "s_nop 0\n\t"
    "ds_write_b64 v66, v[52:53] offset:32768\n\t"
    "v_accvgpr_read_b32 v56, a56\n\t"
    "v_accvgpr_read_b32 v57, a57\n\t"
    "v_accvgpr_read_b32 v58, a58\n\t"
    "v_accvgpr_read_b32 v59, a59\n\t"
    "v_pk_mul_f32 v[56:57], v[56:57], v[188:189] op_sel_hi:[1,0]\n\t"
    "v_pk_mul_f32 v[58:59], v[58:59], v[188:189] op_sel_hi:[1,0]\n\t"
    "v_cvt_pk_bf16_f32 v60, v56, v57\n\t"
    "v_cvt_pk_bf16_f32 v61, v58, v59\n\t"
    "s_nop 0\n\t"
    "ds_write_b64 v67, v[60:61] offset:32768\n\t"
    "v_accvgpr_read_b32 v32, a72\n\t"
    "v_accvgpr_read_b32 v33, a73\n\t"
    "v_accvgpr_read_b32 v34, a74\n\t"
    "v_accvgpr_read_b32 v35, a75\n\t"
    "v_pk_mul_f32 v[32:33], v[32:33], v[188:189] op_sel_hi:[1,0]\n\t"
    "v_pk_mul_f32 v[34:35], v[34:35], v[188:189] op_sel_hi:[1,0]\n\t"
    "v_cvt_pk_bf16_f32 v36, v32, v33\n\t"
    "v_cvt_pk_bf16_f32 v37, v34, v35\n\t"
    "s_nop 0\n\t"
    "ds_write_b64 v68, v[36:37] offset:32768\n\t"
    "v_accvgpr_read_b32 v40, a88\n\t"
    "v_accvgpr_read_b32 v41, a89\n\t"
    "v_accvgpr_read_b32 v42, a90\n\t"
    "v_accvgpr_read_b32 v43, a91\n\t"
    "v_pk_mul_f32 v[40:41], v[40:41], v[188:189] op_sel_hi:[1,0]\n\t"
    "v_pk_mul_f32 v[42:43], v[42:43], v[188:189] op_sel_hi:[1,0]\n\t"
    "v_cvt_pk_bf16_f32 v44, v40, v41\n\t"
    "v_cvt_pk_bf16_f32 v45, v42, v43\n\t"
    "s_nop 0\n\t"
    "ds_write_b64 v69, v[44:45] offset:32768\n\t"
    "v_accvgpr_read_b32 v48, a104\n\t"
    "v_accvgpr_read_b32 v49, a105\n\t"
    "v_accvgpr_read_b32 v50, a106\n\t"
    "v_accvgpr_read_b32 v51, a107\n\t"
    "v_pk_mul_f32 v[48:49], v[48:49], v[188:189] op_sel_hi:[1,0]\n\t"
    "v_pk_mul_f32 v[50:51], v[50:51], v[188:189] op_sel_hi:[1,0]\n\t"
    "v_cvt_pk_bf16_f32 v52, v48, v49\n\t"
    "v_cvt_pk_bf16_f32 v53, v50, v51\n\t"
    "s_nop 0\n\t"
    "ds_write_b64 v70, v[52:53] offset:32768\n\t"
    "v_accvgpr_read_b32 v56, a120\n\t"
    "v_accvgpr_read_b32 v57, a121\n\t"
    "v_accvgpr_read_b32 v58, a122\n\t"
    "v_accvgpr_read_b32 v59, a123\n\t"
    "v_pk_mul_f32 v[56:57], v[56:57], v[188:189] op_sel_hi:[1,0]\n\t"
    "v_pk_mul_f32 v[58:59], v[58:59], v[188:189] op_sel_hi:[1,0]\n\t"
    "v_cvt_pk_bf16_f32 v60, v56, v57\n\t"
    "v_cvt_pk_bf16_f32 v61, v58, v59\n\t"
    "s_nop 0\n\t"
    "ds_write_b64 v71, v[60:61] offset:32768\n\t"
    "s_waitcnt lgkmcnt(0)\n\t"
    "ds_read_b128 v[96:99], v25 offset:32768\n\t"
    "ds_read_b128 v[100:103], v26 offset:33792\n\t"
    "ds_read_b128 v[104:107], v27 offset:34816\n\t"
    "ds_read_b128 v[108:111], v28 offset:35840\n\t"
    "v_accvgpr_read_b32 v32, a12\n\t"
    "v_accvgpr_read_b32 v33, a13\n\t"
    "v_accvgpr_read_b32 v34, a14\n\t"
    "v_accvgpr_read_b32 v35, a15\n\t"
    "v_pk_mul_f32 v[32:33], v[32:33], v[30:31] op_sel_hi:[1,0]\n\t"
    "v_pk_mul_f32 v[34:35], v[34:35], v[30:31] op_sel_hi:[1,0]\n\t"
    "v_cvt_pk_bf16_f32 v36, v32, v33\n\t"
    "v_cvt_pk_bf16_f32 v37, v34, v35\n\t"
    "s_nop 0\n\t"
    "ds_write_b64 v64, v[36:37] offset:49152\n\t"
    "v_accvgpr_read_b32 v40, a28\n\t"
    "v_accvgpr_read_b32 v41, a29\n\t"
    "v_accvgpr_read_b32 v42, a30\n\t"
    "v_accvgpr_read_b32 v43, a31\n\t"
    "v_pk_mul_f32 v[40:41], v[40:41], v[30:31] op_sel_hi:[1,0]\n\t"
    "v_pk_mul_f32 v[42:43], v[42:43], v[30:31] op_sel_hi:[1,0]\n\t"
    "v_cvt_pk_bf16_f32 v44, v40, v41\n\t"
    "v_cvt_pk_bf16_f32 v45, v42, v43\n\t"
    "s_nop 0\n\t"
    "ds_write_b64 v65, v[44:45] offset:49152\n\t"
    "s_waitcnt lgkmcnt(2)\n\t"
    "s_add_u32 s66, s64, 8192\n\t"
    "buffer_store_dwordx4 v[96:99], v29, s[52:55], s66 offen sc0 sc1\n\t"
    "buffer_store_dwordx4 v[100:103], v29, s[52:55], s66 offen offset:1024 sc0 sc1\n\t"
    "buffer_store_dwordx4 v[104:107], v29, s[52:55], s66 offen offset:2048 sc0 sc1\n\t"
    "buffer_store_dwordx4 v[108:111], v29, s[52:55], s66 offen offset:3072 sc0 sc1\n\t"
    "v_accvgpr_read_b32 v48, a44\n\t"
    "v_accvgpr_read_b32 v49, a45\n\t"
    "v_accvgpr_read_b32 v50, a46\n\t"
    "v_accvgpr_read_b32 v51, a47\n\t"
    "v_pk_mul_f32 v[48:49], v[48:49], v[30:31] op_sel_hi:[1,0]\n\t"
    "v_pk_mul_f32 v[50:51], v[50:51], v[30:31] op_sel_hi:[1,0]\n\t"
    "v_cvt_pk_bf16_f32 v52, v48, v49\n\t"
    "v_cvt_pk_bf16_f32 v53, v50, v51\n\t"
    "s_nop 0\n\t"
    "ds_write_b64 v66, v[52:53] offset:49152\n\t"
    "v_accvgpr_read_b32 v56, a60\n\t"
    "v_accvgpr_read_b32 v57, a61\n\t"
    "v_accvgpr_read_b32 v58, a62\n\t"
    "v_accvgpr_read_b32 v59, a63\n\t"
    "v_pk_mul_f32 v[56:57], v[56:57], v[30:31] op_sel_hi:[1,0]\n\t"
    "v_pk_mul_f32 v[58:59], v[58:59], v[30:31] op_sel_hi:[1,0]\n\t"
    "v_cvt_pk_bf16_f32 v60, v56, v57\n\t"
    "v_cvt_pk_bf16_f32 v61, v58, v59\n\t"
    "s_nop 0\n\t"
    "ds_write_b64 v67, v[60:61] offset:49152\n\t"
    "v_accvgpr_read_b32 v32, a76\n\t"
    "v_accvgpr_read_b32 v33, a77\n\t"
    "v_accvgpr_read_b32 v34, a78\n\t"
    "v_accvgpr_read_b32 v35, a79\n\t"
    "v_pk_mul_f32 v[32:33], v[32:33], v[30:31] op_sel_hi:[1,0]\n\t"
    "v_pk_mul_f32 v[34:35], v[34:35], v[30:31] op_sel_hi:[1,0]\n\t"
    "v_cvt_pk_bf16_f32 v36, v32, v33\n\t"
    "v_cvt_pk_bf16_f32 v37, v34, v35\n\t"
    "s_nop 0\n\t"
    "ds_write_b64 v68, v[36:37] offset:49152\n\t"
    "v_accvgpr_read_b32 v40, a92\n\t"
    "v_accvgpr_read_b32 v41, a93\n\t"
    "v_accvgpr_read_b32 v42, a94\n\t"
    "v_accvgpr_read_b32 v43, a95\n\t"
    "v_pk_mul_f32 v[40:41], v[40:41], v[30:31] op_sel_hi:[1,0]\n\t"
    "v_pk_mul_f32 v[42:43], v[42:43], v[30:31] op_sel_hi:[1,0]\n\t"
    "v_cvt_pk_bf16_f32 v44, v40, v41\n\t"
    "v_cvt_pk_bf16_f32 v45, v42, v43\n\t"
    "s_nop 0\n\t"
    "ds_write_b64 v69, v[44:45] offset:49152\n\t"
    "v_accvgpr_read_b32 v48, a108\n\t"
    "v_accvgpr_read_b32 v49, a109\n\t"
    "v_accvgpr_read_b32 v50, a110\n\t"
    "v_accvgpr_read_b32 v51, a111\n\t"
    "v_pk_mul_f32 v[48:49], v[48:49], v[30:31] op_sel_hi:[1,0]\n\t"
    "v_pk_mul_f32 v[50:51], v[50:51], v[30:31] op_sel_hi:[1,0]\n\t"
    "v_cvt_pk_bf16_f32 v52, v48, v49\n\t"
    "v_cvt_pk_bf16_f32 v53, v50, v51\n\t"
    "s_nop 0\n\t"
    "ds_write_b64 v70, v[52:53] offset:49152\n\t"
    "v_accvgpr_read_b32 v56, a124\n\t"
    "v_accvgpr_read_b32 v57, a125\n\t"
    "v_accvgpr_read_b32 v58, a126\n\t"
    "v_accvgpr_read_b32 v59, a127\n\t"
    "v_pk_mul_f32 v[56:57], v[56:57], v[30:31] op_sel_hi:[1,0]\n\t"
    "v_pk_mul_f32 v[58:59], v[58:59], v[30:31] op_sel_hi:[1,0]\n\t"
    "v_cvt_pk_bf16_f32 v60, v56, v57\n\t"
    "v_cvt_pk_bf16_f32 v61, v58, v59\n\t"
    "s_nop 0\n\t"
    "ds_write_b64 v71, v[60:61] offset:49152\n\t"
    "s_waitcnt lgkmcnt(0)\n\t"
    "ds_read_b128 v[112:115], v25 offset:49152\n\t"
    "ds_read_b128 v[116:119], v26 offset:50176\n\t"
    "ds_read_b128 v[120:123], v27 offset:51200\n\t"
    "ds_read_b128 v[124:127], v28 offset:52224\n\t"
    "s_waitcnt lgkmcnt(0)\n\t"
    "s_add_u32 s66, s64, 12288\n\t"
    "buffer_store_dwordx4 v[112:115], v29, s[52:55], s66 offen sc0 sc1\n\t"
    "buffer_store_dwordx4 v[116:119], v29, s[52:55], s66 offen offset:1024 sc0 sc1\n\t"
    "buffer_store_dwordx4 v[120:123], v29, s[52:55], s66 offen offset:2048 sc0 sc1\n\t"
    "buffer_store_dwordx4 v[124:127], v29, s[52:55], s66 offen offset:3072 sc0 sc1\n\t"
    "s_branch L_done_%=\n\t"
    "L_rare0_%=:\n\t"
    "s_nop 15\n\t"
    "v_max3_f32 v18, v32, v33, v34\n\t"
    "v_max3_f32 v18, v18, v35, v48\n\t"
    "v_max3_f32 v18, v18, v49, v50\n\t"
    "v_max_f32 v18, v18, v51\n\t"
    "v_mov_b32 v19, v18\n\t"
    "s_nop 1\n\t"
    "v_permlane16_swap_b32 v18, v19\n\t"
    "s_nop 1\n\t"
    "v_max_f32 v18, v18, v19\n\t"
    "v_mov_b32 v19, v18\n\t"
    "s_nop 1\n\t"
    "v_permlane32_swap_b32 v18, v19\n\t"
    "s_nop 1\n\t"
    "v_max_f32 v18, v18, v19\n\t"
    "v_add_f32 v18, 0x40e00000, v18\n\t"
    "v_max_f32 v18, 0, v18\n\t"
    "v_mov_b32 v19, 0x43800000\n\t"
    "v_cmp_le_f32 vcc, v19, v160\n\t"
    "v_mov_b32 v19, 0x40c00000\n\t"
    "s_nop 1\n\t"
    "v_cndmask_b32 v19, 0, v19, vcc\n\t"
    "v_max_f32 v18, v18, v19\n\t"
    "v_exp_f32_e64 v20, -v18\n\t"
    "v_sub_f32 v32, v32, v18\n\t"
    "v_sub_f32 v33, v33, v18\n\t"
    "v_sub_f32 v34, v34, v18\n\t"
    "v_sub_f32 v35, v35, v18\n\t"
    "v_sub_f32 v48, v48, v18\n\t"
    "v_sub_f32 v49, v49, v18\n\t"
    "v_sub_f32 v50, v50, v18\n\t"
    "v_sub_f32 v51, v51, v18\n\t"
    "v_sub_f32 v64, v64, v18\n\t"
    "v_sub_f32 v65, v65, v18\n\t"
    "v_sub_f32 v66, v66, v18\n\t"
    "v_sub_f32 v67, v67, v18\n\t"
    "v_sub_f32 v80, v80, v18\n\t"
    "v_sub_f32 v81, v81, v18\n\t"
    "v_sub_f32 v82, v82, v18\n\t"
    "v_sub_f32 v83, v83, v18\n\t"
    "v_sub_f32 v128, v128, v18\n\t"
    "v_sub_f32 v129, v129, v18\n\t"
    "v_sub_f32 v130, v130, v18\n\t"
    "v_sub_f32 v131, v131, v18\n\t"
    "v_mul_f32 v160, v20, v160\n\t"
    "v_mul_f32 v161, v20, v161\n\t"
    "v_mul_f32 v162, v20, v162\n\t"
    "v_mul_f32 v163, v20, v163\n\t"
    "v_accvgpr_read_b32 v22, a0\n\t"
    "v_accvgpr_read_b32 v23, a1\n\t"
    "v_accvgpr_read_b32 v24, a2\n\t"
    "v_accvgpr_read_b32 v25, a3\n\t"
    "v_mul_f32 v22, v20, v22\n\t"
    "v_mul_f32 v23, v20, v23\n\t"
    "v_mul_f32 v24, v20, v24\n\t"
    "v_mul_f32 v25, v20, v25\n\t"
    "v_accvgpr_write_b32 a0, v22\n\t"
    "v_accvgpr_write_b32 a1, v23\n\t"
    "v_accvgpr_write_b32 a2, v24\n\t"
    "v_accvgpr_write_b32 a3, v25\n\t"
    "v_accvgpr_read_b32 v22, a16\n\t"
    "v_accvgpr_read_b32 v23, a17\n\t"
    "v_accvgpr_read_b32 v24, a18\n\t"
    "v_accvgpr_read_b32 v25, a19\n\t"
    "v_mul_f32 v22, v20, v22\n\t"
    "v_mul_f32 v23, v20, v23\n\t"
    "v_mul_f32 v24, v20, v24\n\t"
    "v_mul_f32 v25, v20, v25\n\t"
    "v_accvgpr_write_b32 a16, v22\n\t"
    "v_accvgpr_write_b32 a17, v23\n\t"
    "v_accvgpr_write_b32 a18, v24\n\t"
    "v_accvgpr_write_b32 a19, v25\n\t"
    "v_accvgpr_read_b32 v22, a32\n\t"
    "v_accvgpr_read_b32 v23, a33\n\t"
    "v_accvgpr_read_b32 v24, a34\n\t"
    "v_accvgpr_read_b32 v25, a35\n\t"
    "v_mul_f32 v22, v20, v22\n\t"
    "v_mul_f32 v23, v20, v23\n\t"
    "v_mul_f32 v24, v20, v24\n\t"
    "v_mul_f32 v25, v20, v25\n\t"
    "v_accvgpr_write_b32 a32, v22\n\t"
    "v_accvgpr_write_b32 a33, v23\n\t"
    "v_accvgpr_write_b32 a34, v24\n\t"
    "v_accvgpr_write_b32 a35, v25\n\t"
    "v_accvgpr_read_b32 v22, a48\n\t"
    "v_accvgpr_read_b32 v23, a49\n\t"
    "v_accvgpr_read_b32 v24, a50\n\t"
    "v_accvgpr_read_b32 v25, a51\n\t"
    "v_mul_f32 v22, v20, v22\n\t"
    "v_mul_f32 v23, v20, v23\n\t"
    "v_mul_f32 v24, v20, v24\n\t"
    "v_mul_f32 v25, v20, v25\n\t"
    "v_accvgpr_write_b32 a48, v22\n\t"
    "v_accvgpr_write_b32 a49, v23\n\t"
    "v_accvgpr_write_b32 a50, v24\n\t"
    "v_accvgpr_write_b32 a51, v25\n\t"
    "v_accvgpr_read_b32 v22, a64\n\t"
    "v_accvgpr_read_b32 v23, a65\n\t"
    "v_accvgpr_read_b32 v24, a66\n\t"
    "v_accvgpr_read_b32 v25, a67\n\t"
    "v_mul_f32 v22, v20, v22\n\t"
    "v_mul_f32 v23, v20, v23\n\t"
    "v_mul_f32 v24, v20, v24\n\t"
    "v_mul_f32 v25, v20, v25\n\t"
    "v_accvgpr_write_b32 a64, v22\n\t"
    "v_accvgpr_write_b32 a65, v23\n\t"
    "v_accvgpr_write_b32 a66, v24\n\t"
    "v_accvgpr_write_b32 a67, v25\n\t"
    "v_accvgpr_read_b32 v22, a80\n\t"
    "v_accvgpr_read_b32 v23, a81\n\t"
    "v_accvgpr_read_b32 v24, a82\n\t"
    "v_accvgpr_read_b32 v25, a83\n\t"
    "v_mul_f32 v22, v20, v22\n\t"
    "v_mul_f32 v23, v20, v23\n\t"
    "v_mul_f32 v24, v20, v24\n\t"
    "v_mul_f32 v25, v20, v25\n\t"
    "v_accvgpr_write_b32 a80, v22\n\t"
    "v_accvgpr_write_b32 a81, v23\n\t"
    "v_accvgpr_write_b32 a82, v24\n\t"
    "v_accvgpr_write_b32 a83, v25\n\t"
    "v_accvgpr_read_b32 v22, a96\n\t"
    "v_accvgpr_read_b32 v23, a97\n\t"
    "v_accvgpr_read_b32 v24, a98\n\t"
    "v_accvgpr_read_b32 v25, a99\n\t"
    "v_mul_f32 v22, v20, v22\n\t"
    "v_mul_f32 v23, v20, v23\n\t"
    "v_mul_f32 v24, v20, v24\n\t"
    "v_mul_f32 v25, v20, v25\n\t"
    "v_accvgpr_write_b32 a96, v22\n\t"
    "v_accvgpr_write_b32 a97, v23\n\t"
    "v_accvgpr_write_b32 a98, v24\n\t"
    "v_accvgpr_write_b32 a99, v25\n\t"
    "v_accvgpr_read_b32 v22, a112\n\t"
    "v_accvgpr_read_b32 v23, a113\n\t"
    "v_accvgpr_read_b32 v24, a114\n\t"
    "v_accvgpr_read_b32 v25, a115\n\t"
    "v_mul_f32 v22, v20, v22\n\t"
    "v_mul_f32 v23, v20, v23\n\t"
    "v_mul_f32 v24, v20, v24\n\t"
    "v_mul_f32 v25, v20, v25\n\t"
    "v_accvgpr_write_b32 a112, v22\n\t"
    "v_accvgpr_write_b32 a113, v23\n\t"
    "v_accvgpr_write_b32 a114, v24\n\t"
    "v_accvgpr_write_b32 a115, v25\n\t"
    "v_exp_f32 v22, v32\n\t"
    "v_exp_f32 v23, v33\n\t"
    "s_nop 0\n\t"
    "v_cvt_pk_bf16_f32 v96, v22, v23\n\t"
    "v_exp_f32 v22, v34\n\t"
    "v_exp_f32 v23, v35\n\t"
    "s_nop 0\n\t"
    "v_cvt_pk_bf16_f32 v97, v22, v23\n\t"
    "v_exp_f32 v22, v48\n\t"
    "v_exp_f32 v23, v49\n\t"
    "s_nop 0\n\t"
    "v_cvt_pk_bf16_f32 v98, v22, v23\n\t"
    "v_exp_f32 v22, v50\n\t"
    "v_exp_f32 v23, v51\n\t"
    "s_nop 0\n\t"
    "v_cvt_pk_bf16_f32 v99, v22, v23\n\t"
    "s_nop 1\n\t"
    "v_mfma_f32_16x16x32_bf16 v[144:147], v[176:179], v[96:99], v[160:163]\n\t"
    "v_max3_f32 v18, v36, v37, v38\n\t"
    "v_max3_f32 v18, v18, v39, v52\n\t"
    "v_max3_f32 v18, v18, v53, v54\n\t"
    "v_max_f32 v18, v18, v55\n\t"
    "v_mov_b32 v19, v18\n\t"
    "s_nop 1\n\t"
    "v_permlane16_swap_b32 v18, v19\n\t"
    "s_nop 1\n\t"
    "v_max_f32 v18, v18, v19\n\t"
    "v_mov_b32 v19, v18\n\t"
    "s_nop 1\n\t"
    "v_permlane32_swap_b32 v18, v19\n\t"
    "s_nop 1\n\t"
    "v_max_f32 v18, v18, v19\n\t"
    "v_add_f32 v18, 0x40e00000, v18\n\t"
    "v_max_f32 v18, 0, v18\n\t"
    "v_mov_b32 v19, 0x43800000\n\t"
    "v_cmp_le_f32 vcc, v19, v164\n\t"
    "v_mov_b32 v19, 0x40c00000\n\t"
    "s_nop 1\n\t"
    "v_cndmask_b32 v19, 0, v19, vcc\n\t"
    "v_max_f32 v18, v18, v19\n\t"
    "v_exp_f32_e64 v20, -v18\n\t"
    "v_sub_f32 v36, v36, v18\n\t"
    "v_sub_f32 v37, v37, v18\n\t"
    "v_sub_f32 v38, v38, v18\n\t"
    "v_sub_f32 v39, v39, v18\n\t"
    "v_sub_f32 v52, v52, v18\n\t"
    "v_sub_f32 v53, v53, v18\n\t"
    "v_sub_f32 v54, v54, v18\n\t"
    "v_sub_f32 v55, v55, v18\n\t"
    "v_sub_f32 v68, v68, v18\n\t"
    "v_sub_f32 v69, v69, v18\n\t"
    "v_sub_f32 v70, v70, v18\n\t"
    "v_sub_f32 v71, v71, v18\n\t"
    "v_sub_f32 v84, v84, v18\n\t"
    "v_sub_f32 v85, v85, v18\n\t"
    "v_sub_f32 v86, v86, v18\n\t"
    "v_sub_f32 v87, v87, v18\n\t"
    "v_sub_f32 v132, v132, v18\n\t"
    "v_sub_f32 v133, v133, v18\n\t"
    "v_sub_f32 v134, v134, v18\n\t"
    "v_sub_f32 v135, v135, v18\n\t"
    "v_mul_f32 v164, v20, v164\n\t"
    "v_mul_f32 v165, v20, v165\n\t"
    "v_mul_f32 v166, v20, v166\n\t"
    "v_mul_f32 v167, v20, v167\n\t"
    "v_accvgpr_read_b32 v22, a4\n\t"
    "v_accvgpr_read_b32 v23, a5\n\t"
    "v_accvgpr_read_b32 v24, a6\n\t"
    "v_accvgpr_read_b32 v25, a7\n\t"
    "v_mul_f32 v22, v20, v22\n\t"
    "v_mul_f32 v23, v20, v23\n\t"
    "v_mul_f32 v24, v20, v24\n\t"
    "v_mul_f32 v25, v20, v25\n\t"
    "v_accvgpr_write_b32 a4, v22\n\t"
    "v_accvgpr_write_b32 a5, v23\n\t"
    "v_accvgpr_write_b32 a6, v24\n\t"
    "v_accvgpr_write_b32 a7, v25\n\t"
    "v_accvgpr_read_b32 v22, a20\n\t"
    "v_accvgpr_read_b32 v23, a21\n\t"
    "v_accvgpr_read_b32 v24, a22\n\t"
    "v_accvgpr_read_b32 v25, a23\n\t"
    "v_mul_f32 v22, v20, v22\n\t"
    "v_mul_f32 v23, v20, v23\n\t"
    "v_mul_f32 v24, v20, v24\n\t"
    "v_mul_f32 v25, v20, v25\n\t"
    "v_accvgpr_write_b32 a20, v22\n\t"
    "v_accvgpr_write_b32 a21, v23\n\t"
    "v_accvgpr_write_b32 a22, v24\n\t"
    "v_accvgpr_write_b32 a23, v25\n\t"
    "v_accvgpr_read_b32 v22, a36\n\t"
    "v_accvgpr_read_b32 v23, a37\n\t"
    "v_accvgpr_read_b32 v24, a38\n\t"
    "v_accvgpr_read_b32 v25, a39\n\t"
    "v_mul_f32 v22, v20, v22\n\t"
    "v_mul_f32 v23, v20, v23\n\t"
    "v_mul_f32 v24, v20, v24\n\t"
    "v_mul_f32 v25, v20, v25\n\t"
    "v_accvgpr_write_b32 a36, v22\n\t"
    "v_accvgpr_write_b32 a37, v23\n\t"
    "v_accvgpr_write_b32 a38, v24\n\t"
    "v_accvgpr_write_b32 a39, v25\n\t"
    "v_accvgpr_read_b32 v22, a52\n\t"
    "v_accvgpr_read_b32 v23, a53\n\t"
    "v_accvgpr_read_b32 v24, a54\n\t"
    "v_accvgpr_read_b32 v25, a55\n\t"
    "v_mul_f32 v22, v20, v22\n\t"
    "v_mul_f32 v23, v20, v23\n\t"
    "v_mul_f32 v24, v20, v24\n\t"
    "v_mul_f32 v25, v20, v25\n\t"
    "v_accvgpr_write_b32 a52, v22\n\t"
    "v_accvgpr_write_b32 a53, v23\n\t"
    "v_accvgpr_write_b32 a54, v24\n\t"
    "v_accvgpr_write_b32 a55, v25\n\t"
    "v_accvgpr_read_b32 v22, a68\n\t"
    "v_accvgpr_read_b32 v23, a69\n\t"
    "v_accvgpr_read_b32 v24, a70\n\t"
    "v_accvgpr_read_b32 v25, a71\n\t"
    "v_mul_f32 v22, v20, v22\n\t"
    "v_mul_f32 v23, v20, v23\n\t"
    "v_mul_f32 v24, v20, v24\n\t"
    "v_mul_f32 v25, v20, v25\n\t"
    "v_accvgpr_write_b32 a68, v22\n\t"
    "v_accvgpr_write_b32 a69, v23\n\t"
    "v_accvgpr_write_b32 a70, v24\n\t"
    "v_accvgpr_write_b32 a71, v25\n\t"
    "v_accvgpr_read_b32 v22, a84\n\t"
    "v_accvgpr_read_b32 v23, a85\n\t"
    "v_accvgpr_read_b32 v24, a86\n\t"
    "v_accvgpr_read_b32 v25, a87\n\t"
    "v_mul_f32 v22, v20, v22\n\t"
    "v_mul_f32 v23, v20, v23\n\t"
    "v_mul_f32 v24, v20, v24\n\t"
    "v_mul_f32 v25, v20, v25\n\t"
    "v_accvgpr_write_b32 a84, v22\n\t"
    "v_accvgpr_write_b32 a85, v23\n\t"
    "v_accvgpr_write_b32 a86, v24\n\t"
    "v_accvgpr_write_b32 a87, v25\n\t"
    "v_accvgpr_read_b32 v22, a100\n\t"
    "v_accvgpr_read_b32 v23, a101\n\t"
    "v_accvgpr_read_b32 v24, a102\n\t"
    "v_accvgpr_read_b32 v25, a103\n\t"
    "v_mul_f32 v22, v20, v22\n\t"
    "v_mul_f32 v23, v20, v23\n\t"
    "v_mul_f32 v24, v20, v24\n\t"
    "v_mul_f32 v25, v20, v25\n\t"
    "v_accvgpr_write_b32 a100, v22\n\t"
    "v_accvgpr_write_b32 a101, v23\n\t"
    "v_accvgpr_write_b32 a102, v24\n\t"
    "v_accvgpr_write_b32 a103, v25\n\t"
    "v_accvgpr_read_b32 v22, a116\n\t"
    "v_accvgpr_read_b32 v23, a117\n\t"
    "v_accvgpr_read_b32 v24, a118\n\t"
    "v_accvgpr_read_b32 v25, a119\n\t"
    "v_mul_f32 v22, v20, v22\n\t"
    "v_mul_f32 v23, v20, v23\n\t"
    "v_mul_f32 v24, v20, v24\n\t"
    "v_mul_f32 v25, v20, v25\n\t"
    "v_accvgpr_write_b32 a116, v22\n\t"
    "v_accvgpr_write_b32 a117, v23\n\t"
    "v_accvgpr_write_b32 a118, v24\n\t"
    "v_accvgpr_write_b32 a119, v25\n\t"
    "v_exp_f32 v22, v36\n\t"
    "v_exp_f32 v23, v37\n\t"
    "s_nop 0\n\t"
    "v_cvt_pk_bf16_f32 v100, v22, v23\n\t"
    "v_exp_f32 v22, v38\n\t"
    "v_exp_f32 v23, v39\n\t"
    "s_nop 0\n\t"
    "v_cvt_pk_bf16_f32 v101, v22, v23\n\t"
    "v_exp_f32 v22, v52\n\t"
    "v_exp_f32 v23, v53\n\t"
    "s_nop 0\n\t"
    "v_cvt_pk_bf16_f32 v102, v22, v23\n\t"
    "v_exp_f32 v22, v54\n\t"
    "v_exp_f32 v23, v55\n\t"
    "s_nop 0\n\t"
    "v_cvt_pk_bf16_f32 v103, v22, v23\n\t"
    "s_nop 1\n\t"
    "v_mfma_f32_16x16x32_bf16 v[148:151], v[176:179], v[100:103], v[164:167]\n\t"
    "v_max3_f32 v18, v40, v41, v42\n\t"
    "v_max3_f32 v18, v18, v43, v56\n\t"
    "v_max3_f32 v18, v18, v57, v58\n\t"
    "v_max_f32 v18, v18, v59\n\t"
    "v_mov_b32 v19, v18\n\t"
    "s_nop 1\n\t"
    "v_permlane16_swap_b32 v18, v19\n\t"
    "s_nop 1\n\t"
    "v_max_f32 v18, v18, v19\n\t"
    "v_mov_b32 v19, v18\n\t"
    "s_nop 1\n\t"
    "v_permlane32_swap_b32 v18, v19\n\t"
    "s_nop 1\n\t"
    "v_max_f32 v18, v18, v19\n\t"
    "v_add_f32 v18, 0x40e00000, v18\n\t"
    "v_max_f32 v18, 0, v18\n\t"
    "v_mov_b32 v19, 0x43800000\n\t"
    "v_cmp_le_f32 vcc, v19, v168\n\t"
    "v_mov_b32 v19, 0x40c00000\n\t"
    "s_nop 1\n\t"
    "v_cndmask_b32 v19, 0, v19, vcc\n\t"
    "v_max_f32 v18, v18, v19\n\t"
    "v_exp_f32_e64 v20, -v18\n\t"
    "v_sub_f32 v40, v40, v18\n\t"
    "v_sub_f32 v41, v41, v18\n\t"
    "v_sub_f32 v42, v42, v18\n\t"
    "v_sub_f32 v43, v43, v18\n\t"
    "v_sub_f32 v56, v56, v18\n\t"
    "v_sub_f32 v57, v57, v18\n\t"
    "v_sub_f32 v58, v58, v18\n\t"
    "v_sub_f32 v59, v59, v18\n\t"
    "v_sub_f32 v72, v72, v18\n\t"
    "v_sub_f32 v73, v73, v18\n\t"
    "v_sub_f32 v74, v74, v18\n\t"
    "v_sub_f32 v75, v75, v18\n\t"
    "v_sub_f32 v88, v88, v18\n\t"
    "v_sub_f32 v89, v89, v18\n\t"
    "v_sub_f32 v90, v90, v18\n\t"
    "v_sub_f32 v91, v91, v18\n\t"
    "v_sub_f32 v136, v136, v18\n\t"
    "v_sub_f32 v137, v137, v18\n\t"
    "v_sub_f32 v138, v138, v18\n\t"
    "v_sub_f32 v139, v139, v18\n\t"
    "v_mul_f32 v168, v20, v168\n\t"
    "v_mul_f32 v169, v20, v169\n\t"
    "v_mul_f32 v170, v20, v170\n\t"
    "v_mul_f32 v171, v20, v171\n\t"
    "v_accvgpr_read_b32 v22, a8\n\t"
    "v_accvgpr_read_b32 v23, a9\n\t"
    "v_accvgpr_read_b32 v24, a10\n\t"
    "v_accvgpr_read_b32 v25, a11\n\t"
    "v_mul_f32 v22, v20, v22\n\t"
    "v_mul_f32 v23, v20, v23\n\t"
    "v_mul_f32 v24, v20, v24\n\t"
    "v_mul_f32 v25, v20, v25\n\t"
    "v_accvgpr_write_b32 a8, v22\n\t"
    "v_accvgpr_write_b32 a9, v23\n\t"
    "v_accvgpr_write_b32 a10, v24\n\t"
    "v_accvgpr_write_b32 a11, v25\n\t"
    "v_accvgpr_read_b32 v22, a24\n\t"
    "v_accvgpr_read_b32 v23, a25\n\t"
    "v_accvgpr_read_b32 v24, a26\n\t"
    "v_accvgpr_read_b32 v25, a27\n\t"
    "v_mul_f32 v22, v20, v22\n\t"
    "v_mul_f32 v23, v20, v23\n\t"
    "v_mul_f32 v24, v20, v24\n\t"
    "v_mul_f32 v25, v20, v25\n\t"
    "v_accvgpr_write_b32 a24, v22\n\t"
    "v_accvgpr_write_b32 a25, v23\n\t"
    "v_accvgpr_write_b32 a26, v24\n\t"
    "v_accvgpr_write_b32 a27, v25\n\t"
    "v_accvgpr_read_b32 v22, a40\n\t"
    "v_accvgpr_read_b32 v23, a41\n\t"
    "v_accvgpr_read_b32 v24, a42\n\t"
    "v_accvgpr_read_b32 v25, a43\n\t"
    "v_mul_f32 v22, v20, v22\n\t"
    "v_mul_f32 v23, v20, v23\n\t"
    "v_mul_f32 v24, v20, v24\n\t"
    "v_mul_f32 v25, v20, v25\n\t"
    "v_accvgpr_write_b32 a40, v22\n\t"
    "v_accvgpr_write_b32 a41, v23\n\t"
    "v_accvgpr_write_b32 a42, v24\n\t"
    "v_accvgpr_write_b32 a43, v25\n\t"
    "v_accvgpr_read_b32 v22, a56\n\t"
    "v_accvgpr_read_b32 v23, a57\n\t"
    "v_accvgpr_read_b32 v24, a58\n\t"
    "v_accvgpr_read_b32 v25, a59\n\t"
    "v_mul_f32 v22, v20, v22\n\t"
    "v_mul_f32 v23, v20, v23\n\t"
    "v_mul_f32 v24, v20, v24\n\t"
    "v_mul_f32 v25, v20, v25\n\t"
    "v_accvgpr_write_b32 a56, v22\n\t"
    "v_accvgpr_write_b32 a57, v23\n\t"
    "v_accvgpr_write_b32 a58, v24\n\t"
    "v_accvgpr_write_b32 a59, v25\n\t"
    "v_accvgpr_read_b32 v22, a72\n\t"
    "v_accvgpr_read_b32 v23, a73\n\t"
    "v_accvgpr_read_b32 v24, a74\n\t"
    "v_accvgpr_read_b32 v25, a75\n\t"
    "v_mul_f32 v22, v20, v22\n\t"
    "v_mul_f32 v23, v20, v23\n\t"
    "v_mul_f32 v24, v20, v24\n\t"
    "v_mul_f32 v25, v20, v25\n\t"
    "v_accvgpr_write_b32 a72, v22\n\t"
    "v_accvgpr_write_b32 a73, v23\n\t"
    "v_accvgpr_write_b32 a74, v24\n\t"
    "v_accvgpr_write_b32 a75, v25\n\t"
    "v_accvgpr_read_b32 v22, a88\n\t"
    "v_accvgpr_read_b32 v23, a89\n\t"
    "v_accvgpr_read_b32 v24, a90\n\t"
    "v_accvgpr_read_b32 v25, a91\n\t"
    "v_mul_f32 v22, v20, v22\n\t"
    "v_mul_f32 v23, v20, v23\n\t"
    "v_mul_f32 v24, v20, v24\n\t"
    "v_mul_f32 v25, v20, v25\n\t"
    "v_accvgpr_write_b32 a88, v22\n\t"
    "v_accvgpr_write_b32 a89, v23\n\t"
    "v_accvgpr_write_b32 a90, v24\n\t"
    "v_accvgpr_write_b32 a91, v25\n\t"
    "v_accvgpr_read_b32 v22, a104\n\t"
    "v_accvgpr_read_b32 v23, a105\n\t"
    "v_accvgpr_read_b32 v24, a106\n\t"
    "v_accvgpr_read_b32 v25, a107\n\t"
    "v_mul_f32 v22, v20, v22\n\t"
    "v_mul_f32 v23, v20, v23\n\t"
    "v_mul_f32 v24, v20, v24\n\t"
    "v_mul_f32 v25, v20, v25\n\t"
    "v_accvgpr_write_b32 a104, v22\n\t"
    "v_accvgpr_write_b32 a105, v23\n\t"
    "v_accvgpr_write_b32 a106, v24\n\t"
    "v_accvgpr_write_b32 a107, v25\n\t"
    "v_accvgpr_read_b32 v22, a120\n\t"
    "v_accvgpr_read_b32 v23, a121\n\t"
    "v_accvgpr_read_b32 v24, a122\n\t"
    "v_accvgpr_read_b32 v25, a123\n\t"
    "v_mul_f32 v22, v20, v22\n\t"
    "v_mul_f32 v23, v20, v23\n\t"
    "v_mul_f32 v24, v20, v24\n\t"
    "v_mul_f32 v25, v20, v25\n\t"
    "v_accvgpr_write_b32 a120, v22\n\t"
    "v_accvgpr_write_b32 a121, v23\n\t"
    "v_accvgpr_write_b32 a122, v24\n\t"
    "v_accvgpr_write_b32 a123, v25\n\t"
    "v_exp_f32 v22, v40\n\t"
    "v_exp_f32 v23, v41\n\t"
    "s_nop 0\n\t"
    "v_cvt_pk_bf16_f32 v104, v22, v23\n\t"
    "v_exp_f32 v22, v42\n\t"
    "v_exp_f32 v23, v43\n\t"
    "s_nop 0\n\t"
    "v_cvt_pk_bf16_f32 v105, v22, v23\n\t"
    "v_exp_f32 v22, v56\n\t"
    "v_exp_f32 v23, v57\n\t"
    "s_nop 0\n\t"
    "v_cvt_pk_bf16_f32 v106, v22, v23\n\t"
    "v_exp_f32 v22, v58\n\t"
    "v_exp_f32 v23, v59\n\t"
    "s_nop 0\n\t"
    "v_cvt_pk_bf16_f32 v107, v22, v23\n\t"
    "s_nop 1\n\t"
    "v_mfma_f32_16x16x32_bf16 v[152:155], v[176:179], v[104:107], v[168:171]\n\t"
    "v_max3_f32 v18, v44, v45, v46\n\t"
    "v_max3_f32 v18, v18, v47, v60\n\t"
    "v_max3_f32 v18, v18, v61, v62\n\t"
    "v_max_f32 v18, v18, v63\n\t"
    "v_mov_b32 v19, v18\n\t"
    "s_nop 1\n\t"
    "v_permlane16_swap_b32 v18, v19\n\t"
    "s_nop 1\n\t"
    "v_max_f32 v18, v18, v19\n\t"
    "v_mov_b32 v19, v18\n\t"
    "s_nop 1\n\t"
    "v_permlane32_swap_b32 v18, v19\n\t"
    "s_nop 1\n\t"
    "v_max_f32 v18, v18, v19\n\t"
    "v_add_f32 v18, 0x40e00000, v18\n\t"
    "v_max_f32 v18, 0, v18\n\t"
    "v_mov_b32 v19, 0x43800000\n\t"
    "v_cmp_le_f32 vcc, v19, v172\n\t"
    "v_mov_b32 v19, 0x40c00000\n\t"
    "s_nop 1\n\t"
    "v_cndmask_b32 v19, 0, v19, vcc\n\t"
    "v_max_f32 v18, v18, v19\n\t"
    "v_exp_f32_e64 v20, -v18\n\t"
    "v_sub_f32 v44, v44, v18\n\t"
    "v_sub_f32 v45, v45, v18\n\t"
    "v_sub_f32 v46, v46, v18\n\t"
    "v_sub_f32 v47, v47, v18\n\t"
    "v_sub_f32 v60, v60, v18\n\t"
    "v_sub_f32 v61, v61, v18\n\t"
    "v_sub_f32 v62, v62, v18\n\t"
    "v_sub_f32 v63, v63, v18\n\t"
    "v_sub_f32 v76, v76, v18\n\t"
    "v_sub_f32 v77, v77, v18\n\t"
    "v_sub_f32 v78, v78, v18\n\t"
    "v_sub_f32 v79, v79, v18\n\t"
    "v_sub_f32 v92, v92, v18\n\t"
    "v_sub_f32 v93, v93, v18\n\t"
    "v_sub_f32 v94, v94, v18\n\t"
    "v_sub_f32 v95, v95, v18\n\t"
    "v_sub_f32 v140, v140, v18\n\t"
    "v_sub_f32 v141, v141, v18\n\t"
    "v_sub_f32 v142, v142, v18\n\t"
    "v_sub_f32 v143, v143, v18\n\t"
    "v_mul_f32 v172, v20, v172\n\t"
    "v_mul_f32 v173, v20, v173\n\t"
    "v_mul_f32 v174, v20, v174\n\t"
    "v_mul_f32 v175, v20, v175\n\t"
    "v_accvgpr_read_b32 v22, a12\n\t"
    "v_accvgpr_read_b32 v23, a13\n\t"
    "v_accvgpr_read_b32 v24, a14\n\t"
    "v_accvgpr_read_b32 v25, a15\n\t"
    "v_mul_f32 v22, v20, v22\n\t"
    "v_mul_f32 v23, v20, v23\n\t"
    "v_mul_f32 v24, v20, v24\n\t"
    "v_mul_f32 v25, v20, v25\n\t"
    "v_accvgpr_write_b32 a12, v22\n\t"
    "v_accvgpr_write_b32 a13, v23\n\t"
    "v_accvgpr_write_b32 a14, v24\n\t"
    "v_accvgpr_write_b32 a15, v25\n\t"
    "v_accvgpr_read_b32 v22, a28\n\t"
    "v_accvgpr_read_b32 v23, a29\n\t"
    "v_accvgpr_read_b32 v24, a30\n\t"
    "v_accvgpr_read_b32 v25, a31\n\t"
    "v_mul_f32 v22, v20, v22\n\t"
    "v_mul_f32 v23, v20, v23\n\t"
    "v_mul_f32 v24, v20, v24\n\t"
    "v_mul_f32 v25, v20, v25\n\t"
    "v_accvgpr_write_b32 a28, v22\n\t"
    "v_accvgpr_write_b32 a29, v23\n\t"
    "v_accvgpr_write_b32 a30, v24\n\t"
    "v_accvgpr_write_b32 a31, v25\n\t"
    "v_accvgpr_read_b32 v22, a44\n\t"
    "v_accvgpr_read_b32 v23, a45\n\t"
    "v_accvgpr_read_b32 v24, a46\n\t"
    "v_accvgpr_read_b32 v25, a47\n\t"
    "v_mul_f32 v22, v20, v22\n\t"
    "v_mul_f32 v23, v20, v23\n\t"
    "v_mul_f32 v24, v20, v24\n\t"
    "v_mul_f32 v25, v20, v25\n\t"
    "v_accvgpr_write_b32 a44, v22\n\t"
    "v_accvgpr_write_b32 a45, v23\n\t"
    "v_accvgpr_write_b32 a46, v24\n\t"
    "v_accvgpr_write_b32 a47, v25\n\t"
    "v_accvgpr_read_b32 v22, a60\n\t"
    "v_accvgpr_read_b32 v23, a61\n\t"
    "v_accvgpr_read_b32 v24, a62\n\t"
    "v_accvgpr_read_b32 v25, a63\n\t"
    "v_mul_f32 v22, v20, v22\n\t"
    "v_mul_f32 v23, v20, v23\n\t"
    "v_mul_f32 v24, v20, v24\n\t"
    "v_mul_f32 v25, v20, v25\n\t"
    "v_accvgpr_write_b32 a60, v22\n\t"
    "v_accvgpr_write_b32 a61, v23\n\t"
    "v_accvgpr_write_b32 a62, v24\n\t"
    "v_accvgpr_write_b32 a63, v25\n\t"
    "v_accvgpr_read_b32 v22, a76\n\t"
    "v_accvgpr_read_b32 v23, a77\n\t"
    "v_accvgpr_read_b32 v24, a78\n\t"
    "v_accvgpr_read_b32 v25, a79\n\t"
    "v_mul_f32 v22, v20, v22\n\t"
    "v_mul_f32 v23, v20, v23\n\t"
    "v_mul_f32 v24, v20, v24\n\t"
    "v_mul_f32 v25, v20, v25\n\t"
    "v_accvgpr_write_b32 a76, v22\n\t"
    "v_accvgpr_write_b32 a77, v23\n\t"
    "v_accvgpr_write_b32 a78, v24\n\t"
    "v_accvgpr_write_b32 a79, v25\n\t"
    "v_accvgpr_read_b32 v22, a92\n\t"
    "v_accvgpr_read_b32 v23, a93\n\t"
    "v_accvgpr_read_b32 v24, a94\n\t"
    "v_accvgpr_read_b32 v25, a95\n\t"
    "v_mul_f32 v22, v20, v22\n\t"
    "v_mul_f32 v23, v20, v23\n\t"
    "v_mul_f32 v24, v20, v24\n\t"
    "v_mul_f32 v25, v20, v25\n\t"
    "v_accvgpr_write_b32 a92, v22\n\t"
    "v_accvgpr_write_b32 a93, v23\n\t"
    "v_accvgpr_write_b32 a94, v24\n\t"
    "v_accvgpr_write_b32 a95, v25\n\t"
    "v_accvgpr_read_b32 v22, a108\n\t"
    "v_accvgpr_read_b32 v23, a109\n\t"
    "v_accvgpr_read_b32 v24, a110\n\t"
    "v_accvgpr_read_b32 v25, a111\n\t"
    "v_mul_f32 v22, v20, v22\n\t"
    "v_mul_f32 v23, v20, v23\n\t"
    "v_mul_f32 v24, v20, v24\n\t"
    "v_mul_f32 v25, v20, v25\n\t"
    "v_accvgpr_write_b32 a108, v22\n\t"
    "v_accvgpr_write_b32 a109, v23\n\t"
    "v_accvgpr_write_b32 a110, v24\n\t"
    "v_accvgpr_write_b32 a111, v25\n\t"
    "v_accvgpr_read_b32 v22, a124\n\t"
    "v_accvgpr_read_b32 v23, a125\n\t"
    "v_accvgpr_read_b32 v24, a126\n\t"
    "v_accvgpr_read_b32 v25, a127\n\t"
    "v_mul_f32 v22, v20, v22\n\t"
    "v_mul_f32 v23, v20, v23\n\t"
    "v_mul_f32 v24, v20, v24\n\t"
    "v_mul_f32 v25, v20, v25\n\t"
    "v_accvgpr_write_b32 a124, v22\n\t"
    "v_accvgpr_write_b32 a125, v23\n\t"
    "v_accvgpr_write_b32 a126, v24\n\t"
    "v_accvgpr_write_b32 a127, v25\n\t"
    "v_exp_f32 v22, v44\n\t"
    "v_exp_f32 v23, v45\n\t"
    "s_nop 0\n\t"
    "v_cvt_pk_bf16_f32 v108, v22, v23\n\t"
    "v_exp_f32 v22, v46\n\t"
    "v_exp_f32 v23, v47\n\t"
    "s_nop 0\n\t"
    "v_cvt_pk_bf16_f32 v109, v22, v23\n\t"
    "v_exp_f32 v22, v60\n\t"
    "v_exp_f32 v23, v61\n\t"
    "s_nop 0\n\t"
    "v_cvt_pk_bf16_f32 v110, v22, v23\n\t"
    "v_exp_f32 v22, v62\n\t"
    "v_exp_f32 v23, v63\n\t"
    "s_nop 0\n\t"
    "v_cvt_pk_bf16_f32 v111, v22, v23\n\t"
    "s_nop 1\n\t"
    "v_mfma_f32_16x16x32_bf16 v[156:159], v[176:179], v[108:111], v[172:175]\n\t"
    "s_nop 7\n\t"
    "s_branch L_ret0_%=\n\t"
    "L_rare1_%=:\n\t"
    "s_nop 15\n\t"
    "v_max3_f32 v18, v64, v65, v66\n\t"
    "v_max3_f32 v18, v18, v67, v80\n\t"
    "v_max3_f32 v18, v18, v81, v82\n\t"
    "v_max_f32 v18, v18, v83\n\t"
    "v_mov_b32 v19, v18\n\t"
    "s_nop 1\n\t"
    "v_permlane16_swap_b32 v18, v19\n\t"
    "s_nop 1\n\t"
    "v_max_f32 v18, v18, v19\n\t"
    "v_mov_b32 v19, v18\n\t"
    "s_nop 1\n\t"
    "v_permlane32_swap_b32 v18, v19\n\t"
    "s_nop 1\n\t"
    "v_max_f32 v18, v18, v19\n\t"
    "v_add_f32 v18, 0x40e00000, v18\n\t"
    "v_max_f32 v18, 0, v18\n\t"
    "v_mov_b32 v19, 0x43800000\n\t"
    "v_cmp_le_f32 vcc, v19, v144\n\t"
    "v_mov_b32 v19, 0x40c00000\n\t"
    "s_nop 1\n\t"
    "v_cndmask_b32 v19, 0, v19, vcc\n\t"
    "v_max_f32 v18, v18, v19\n\t"
    "v_exp_f32_e64 v20, -v18\n\t"
    "v_sub_f32 v64, v64, v18\n\t"
    "v_sub_f32 v65, v65, v18\n\t"
    "v_sub_f32 v66, v66, v18\n\t"
    "v_sub_f32 v67, v67, v18\n\t"
    "v_sub_f32 v80, v80, v18\n\t"
    "v_sub_f32 v81, v81, v18\n\t"
    "v_sub_f32 v82, v82, v18\n\t"
    "v_sub_f32 v83, v83, v18\n\t"
    "v_sub_f32 v32, v32, v18\n\t"
    "v_sub_f32 v33, v33, v18\n\t"
    "v_sub_f32 v34, v34, v18\n\t"
    "v_sub_f32 v35, v35, v18\n\t"
    "v_sub_f32 v48, v48, v18\n\t"
    "v_sub_f32 v49, v49, v18\n\t"
    "v_sub_f32 v50, v50, v18\n\t"
    "v_sub_f32 v51, v51, v18\n\t"
    "v_sub_f32 v128, v128, v18\n\t"
    "v_sub_f32 v129, v129, v18\n\t"
    "v_sub_f32 v130, v130, v18\n\t"
    "v_sub_f32 v131, v131, v18\n\t"
    "v_mul_f32 v144, v20, v144\n\t"
    "v_mul_f32 v145, v20, v145\n\t"
    "v_mul_f32 v146, v20, v146\n\t"
    "v_mul_f32 v147, v20, v147\n\t"
    "v_accvgpr_read_b32 v22, a0\n\t"
    "v_accvgpr_read_b32 v23, a1\n\t"
    "v_accvgpr_read_b32 v24, a2\n\t"
    "v_accvgpr_read_b32 v25, a3\n\t"
    "v_mul_f32 v22, v20, v22\n\t"
    "v_mul_f32 v23, v20, v23\n\t"
    "v_mul_f32 v24, v20, v24\n\t"
    "v_mul_f32 v25, v20, v25\n\t"
    "v_accvgpr_write_b32 a0, v22\n\t"
    "v_accvgpr_write_b32 a1, v23\n\t"
    "v_accvgpr_write_b32 a2, v24\n\t"
    "v_accvgpr_write_b32 a3, v25\n\t"
    "v_accvgpr_read_b32 v22, a16\n\t"
    "v_accvgpr_read_b32 v23, a17\n\t"
    "v_accvgpr_read_b32 v24, a18\n\t"
    "v_accvgpr_read_b32 v25, a19\n\t"
    "v_mul_f32 v22, v20, v22\n\t"
    "v_mul_f32 v23, v20, v23\n\t"
    "v_mul_f32 v24, v20, v24\n\t"
    "v_mul_f32 v25, v20, v25\n\t"
    "v_accvgpr_write_b32 a16, v22\n\t"
    "v_accvgpr_write_b32 a17, v23\n\t"
    "v_accvgpr_write_b32 a18, v24\n\t"
    "v_accvgpr_write_b32 a19, v25\n\t"
    "v_accvgpr_read_b32 v22, a32\n\t"
    "v_accvgpr_read_b32 v23, a33\n\t"
    "v_accvgpr_read_b32 v24, a34\n\t"
    "v_accvgpr_read_b32 v25, a35\n\t"
    "v_mul_f32 v22, v20, v22\n\t"
    "v_mul_f32 v23, v20, v23\n\t"
    "v_mul_f32 v24, v20, v24\n\t"
    "v_mul_f32 v25, v20, v25\n\t"
    "v_accvgpr_write_b32 a32, v22\n\t"
    "v_accvgpr_write_b32 a33, v23\n\t"
    "v_accvgpr_write_b32 a34, v24\n\t"
    "v_accvgpr_write_b32 a35, v25\n\t"
    "v_accvgpr_read_b32 v22, a48\n\t"
    "v_accvgpr_read_b32 v23, a49\n\t"
    "v_accvgpr_read_b32 v24, a50\n\t"
    "v_accvgpr_read_b32 v25, a51\n\t"
    "v_mul_f32 v22, v20, v22\n\t"
    "v_mul_f32 v23, v20, v23\n\t"
    "v_mul_f32 v24, v20, v24\n\t"
    "v_mul_f32 v25, v20, v25\n\t"
    "v_accvgpr_write_b32 a48, v22\n\t"
    "v_accvgpr_write_b32 a49, v23\n\t"
    "v_accvgpr_write_b32 a50, v24\n\t"
    "v_accvgpr_write_b32 a51, v25\n\t"
    "v_accvgpr_read_b32 v22, a64\n\t"
    "v_accvgpr_read_b32 v23, a65\n\t"
    "v_accvgpr_read_b32 v24, a66\n\t"
    "v_accvgpr_read_b32 v25, a67\n\t"
    "v_mul_f32 v22, v20, v22\n\t"
    "v_mul_f32 v23, v20, v23\n\t"
    "v_mul_f32 v24, v20, v24\n\t"
    "v_mul_f32 v25, v20, v25\n\t"
    "v_accvgpr_write_b32 a64, v22\n\t"
    "v_accvgpr_write_b32 a65, v23\n\t"
    "v_accvgpr_write_b32 a66, v24\n\t"
    "v_accvgpr_write_b32 a67, v25\n\t"
    "v_accvgpr_read_b32 v22, a80\n\t"
    "v_accvgpr_read_b32 v23, a81\n\t"
    "v_accvgpr_read_b32 v24, a82\n\t"
    "v_accvgpr_read_b32 v25, a83\n\t"
    "v_mul_f32 v22, v20, v22\n\t"
    "v_mul_f32 v23, v20, v23\n\t"
    "v_mul_f32 v24, v20, v24\n\t"
    "v_mul_f32 v25, v20, v25\n\t"
    "v_accvgpr_write_b32 a80, v22\n\t"
    "v_accvgpr_write_b32 a81, v23\n\t"
    "v_accvgpr_write_b32 a82, v24\n\t"
    "v_accvgpr_write_b32 a83, v25\n\t"
    "v_accvgpr_read_b32 v22, a96\n\t"
    "v_accvgpr_read_b32 v23, a97\n\t"
    "v_accvgpr_read_b32 v24, a98\n\t"
    "v_accvgpr_read_b32 v25, a99\n\t"
    "v_mul_f32 v22, v20, v22\n\t"
    "v_mul_f32 v23, v20, v23\n\t"
    "v_mul_f32 v24, v20, v24\n\t"
    "v_mul_f32 v25, v20, v25\n\t"
    "v_accvgpr_write_b32 a96, v22\n\t"
    "v_accvgpr_write_b32 a97, v23\n\t"
    "v_accvgpr_write_b32 a98, v24\n\t"
    "v_accvgpr_write_b32 a99, v25\n\t"
    "v_accvgpr_read_b32 v22, a112\n\t"
    "v_accvgpr_read_b32 v23, a113\n\t"
    "v_accvgpr_read_b32 v24, a114\n\t"
    "v_accvgpr_read_b32 v25, a115\n\t"
    "v_mul_f32 v22, v20, v22\n\t"
    "v_mul_f32 v23, v20, v23\n\t"
    "v_mul_f32 v24, v20, v24\n\t"
    "v_mul_f32 v25, v20, v25\n\t"
    "v_accvgpr_write_b32 a112, v22\n\t"
    "v_accvgpr_write_b32 a113, v23\n\t"
    "v_accvgpr_write_b32 a114, v24\n\t"
    "v_accvgpr_write_b32 a115, v25\n\t"
    "v_exp_f32 v22, v64\n\t"
    "v_exp_f32 v23, v65\n\t"
    "s_nop 0\n\t"
    "v_cvt_pk_bf16_f32 v112, v22, v23\n\t"
    "v_exp_f32 v22, v66\n\t"
    "v_exp_f32 v23, v67\n\t"
    "s_nop 0\n\t"
    "v_cvt_pk_bf16_f32 v113, v22, v23\n\t"
    "v_exp_f32 v22, v80\n\t"
    "v_exp_f32 v23, v81\n\t"
    "s_nop 0\n\t"
    "v_cvt_pk_bf16_f32 v114, v22, v23\n\t"
    "v_exp_f32 v22, v82\n\t"
    "v_exp_f32 v23, v83\n\t"
    "s_nop 0\n\t"
    "v_cvt_pk_bf16_f32 v115, v22, v23\n\t"
    "s_nop 1\n\t"
    "v_mfma_f32_16x16x32_bf16 v[160:163], v[176:179], v[112:115], v[144:147]\n\t"
    "v_max3_f32 v18, v68, v69, v70\n\t"
    "v_max3_f32 v18, v18, v71, v84\n\t"
    "v_max3_f32 v18, v18, v85, v86\n\t"
    "v_max_f32 v18, v18, v87\n\t"
    "v_mov_b32 v19, v18\n\t"
    "s_nop 1\n\t"
    "v_permlane16_swap_b32 v18, v19\n\t"
    "s_nop 1\n\t"
    "v_max_f32 v18, v18, v19\n\t"
    "v_mov_b32 v19, v18\n\t"
    "s_nop 1\n\t"
    "v_permlane32_swap_b32 v18, v19\n\t"
    "s_nop 1\n\t"
    "v_max_f32 v18, v18, v19\n\t"
    "v_add_f32 v18, 0x40e00000, v18\n\t"
    "v_max_f32 v18, 0, v18\n\t"
    "v_mov_b32 v19, 0x43800000\n\t"
    "v_cmp_le_f32 vcc, v19, v148\n\t"
    "v_mov_b32 v19, 0x40c00000\n\t"
    "s_nop 1\n\t"
    "v_cndmask_b32 v19, 0, v19, vcc\n\t"
    "v_max_f32 v18, v18, v19\n\t"
    "v_exp_f32_e64 v20, -v18\n\t"
    "v_sub_f32 v68, v68, v18\n\t"
    "v_sub_f32 v69, v69, v18\n\t"
    "v_sub_f32 v70, v70, v18\n\t"
    "v_sub_f32 v71, v71, v18\n\t"
    "v_sub_f32 v84, v84, v18\n\t"
    "v_sub_f32 v85, v85, v18\n\t"
    "v_sub_f32 v86, v86, v18\n\t"
    "v_sub_f32 v87, v87, v18\n\t"
    "v_sub_f32 v36, v36, v18\n\t"
    "v_sub_f32 v37, v37, v18\n\t"
    "v_sub_f32 v38, v38, v18\n\t"
    "v_sub_f32 v39, v39, v18\n\t"
    "v_sub_f32 v52, v52, v18\n\t"
    "v_sub_f32 v53, v53, v18\n\t"
    "v_sub_f32 v54, v54, v18\n\t"
    "v_sub_f32 v55, v55, v18\n\t"
    "v_sub_f32 v132, v132, v18\n\t"
    "v_sub_f32 v133, v133, v18\n\t"
    "v_sub_f32 v134, v134, v18\n\t"
    "v_sub_f32 v135, v135, v18\n\t"
    "v_mul_f32 v148, v20, v148\n\t"
    "v_mul_f32 v149, v20, v149\n\t"
    "v_mul_f32 v150, v20, v150\n\t"
    "v_mul_f32 v151, v20, v151\n\t"
    "v_accvgpr_read_b32 v22, a4\n\t"
    "v_accvgpr_read_b32 v23, a5\n\t"
    "v_accvgpr_read_b32 v24, a6\n\t"
    "v_accvgpr_read_b32 v25, a7\n\t"
    "v_mul_f32 v22, v20, v22\n\t"
    "v_mul_f32 v23, v20, v23\n\t"
    "v_mul_f32 v24, v20, v24\n\t"
    "v_mul_f32 v25, v20, v25\n\t"
    "v_accvgpr_write_b32 a4, v22\n\t"
    "v_accvgpr_write_b32 a5, v23\n\t"
    "v_accvgpr_write_b32 a6, v24\n\t"
    "v_accvgpr_write_b32 a7, v25\n\t"
    "v_accvgpr_read_b32 v22, a20\n\t"
    "v_accvgpr_read_b32 v23, a21\n\t"
    "v_accvgpr_read_b32 v24, a22\n\t"
    "v_accvgpr_read_b32 v25, a23\n\t"
    "v_mul_f32 v22, v20, v22\n\t"
    "v_mul_f32 v23, v20, v23\n\t"
    "v_mul_f32 v24, v20, v24\n\t"
    "v_mul_f32 v25, v20, v25\n\t"
    "v_accvgpr_write_b32 a20, v22\n\t"
    "v_accvgpr_write_b32 a21, v23\n\t"
    "v_accvgpr_write_b32 a22, v24\n\t"
    "v_accvgpr_write_b32 a23, v25\n\t"
    "v_accvgpr_read_b32 v22, a36\n\t"
    "v_accvgpr_read_b32 v23, a37\n\t"
    "v_accvgpr_read_b32 v24, a38\n\t"
    "v_accvgpr_read_b32 v25, a39\n\t"
    "v_mul_f32 v22, v20, v22\n\t"
    "v_mul_f32 v23, v20, v23\n\t"
    "v_mul_f32 v24, v20, v24\n\t"
    "v_mul_f32 v25, v20, v25\n\t"
    "v_accvgpr_write_b32 a36, v22\n\t"
    "v_accvgpr_write_b32 a37, v23\n\t"
    "v_accvgpr_write_b32 a38, v24\n\t"
    "v_accvgpr_write_b32 a39, v25\n\t"
    "v_accvgpr_read_b32 v22, a52\n\t"
    "v_accvgpr_read_b32 v23, a53\n\t"
    "v_accvgpr_read_b32 v24, a54\n\t"
    "v_accvgpr_read_b32 v25, a55\n\t"
    "v_mul_f32 v22, v20, v22\n\t"
    "v_mul_f32 v23, v20, v23\n\t"
    "v_mul_f32 v24, v20, v24\n\t"
    "v_mul_f32 v25, v20, v25\n\t"
    "v_accvgpr_write_b32 a52, v22\n\t"
    "v_accvgpr_write_b32 a53, v23\n\t"
    "v_accvgpr_write_b32 a54, v24\n\t"
    "v_accvgpr_write_b32 a55, v25\n\t"
    "v_accvgpr_read_b32 v22, a68\n\t"
    "v_accvgpr_read_b32 v23, a69\n\t"
    "v_accvgpr_read_b32 v24, a70\n\t"
    "v_accvgpr_read_b32 v25, a71\n\t"
    "v_mul_f32 v22, v20, v22\n\t"
    "v_mul_f32 v23, v20, v23\n\t"
    "v_mul_f32 v24, v20, v24\n\t"
    "v_mul_f32 v25, v20, v25\n\t"
    "v_accvgpr_write_b32 a68, v22\n\t"
    "v_accvgpr_write_b32 a69, v23\n\t"
    "v_accvgpr_write_b32 a70, v24\n\t"
    "v_accvgpr_write_b32 a71, v25\n\t"
    "v_accvgpr_read_b32 v22, a84\n\t"
    "v_accvgpr_read_b32 v23, a85\n\t"
    "v_accvgpr_read_b32 v24, a86\n\t"
    "v_accvgpr_read_b32 v25, a87\n\t"
    "v_mul_f32 v22, v20, v22\n\t"
    "v_mul_f32 v23, v20, v23\n\t"
    "v_mul_f32 v24, v20, v24\n\t"
    "v_mul_f32 v25, v20, v25\n\t"
    "v_accvgpr_write_b32 a84, v22\n\t"
    "v_accvgpr_write_b32 a85, v23\n\t"
    "v_accvgpr_write_b32 a86, v24\n\t"
    "v_accvgpr_write_b32 a87, v25\n\t"
    "v_accvgpr_read_b32 v22, a100\n\t"
    "v_accvgpr_read_b32 v23, a101\n\t"
    "v_accvgpr_read_b32 v24, a102\n\t"
    "v_accvgpr_read_b32 v25, a103\n\t"
    "v_mul_f32 v22, v20, v22\n\t"
    "v_mul_f32 v23, v20, v23\n\t"
    "v_mul_f32 v24, v20, v24\n\t"
    "v_mul_f32 v25, v20, v25\n\t"
    "v_accvgpr_write_b32 a100, v22\n\t"
    "v_accvgpr_write_b32 a101, v23\n\t"
    "v_accvgpr_write_b32 a102, v24\n\t"
    "v_accvgpr_write_b32 a103, v25\n\t"
    "v_accvgpr_read_b32 v22, a116\n\t"
    "v_accvgpr_read_b32 v23, a117\n\t"
    "v_accvgpr_read_b32 v24, a118\n\t"
    "v_accvgpr_read_b32 v25, a119\n\t"
    "v_mul_f32 v22, v20, v22\n\t"
    "v_mul_f32 v23, v20, v23\n\t"
    "v_mul_f32 v24, v20, v24\n\t"
    "v_mul_f32 v25, v20, v25\n\t"
    "v_accvgpr_write_b32 a116, v22\n\t"
    "v_accvgpr_write_b32 a117, v23\n\t"
    "v_accvgpr_write_b32 a118, v24\n\t"
    "v_accvgpr_write_b32 a119, v25\n\t"
    "v_exp_f32 v22, v68\n\t"
    "v_exp_f32 v23, v69\n\t"
    "s_nop 0\n\t"
    "v_cvt_pk_bf16_f32 v116, v22, v23\n\t"
    "v_exp_f32 v22, v70\n\t"
    "v_exp_f32 v23, v71\n\t"
    "s_nop 0\n\t"
    "v_cvt_pk_bf16_f32 v117, v22, v23\n\t"
    "v_exp_f32 v22, v84\n\t"
    "v_exp_f32 v23, v85\n\t"
    "s_nop 0\n\t"
    "v_cvt_pk_bf16_f32 v118, v22, v23\n\t"
    "v_exp_f32 v22, v86\n\t"
    "v_exp_f32 v23, v87\n\t"
    "s_nop 0\n\t"
    "v_cvt_pk_bf16_f32 v119, v22, v23\n\t"
    "s_nop 1\n\t"
    "v_mfma_f32_16x16x32_bf16 v[164:167], v[176:179], v[116:119], v[148:151]\n\t"
    "v_max3_f32 v18, v72, v73, v74\n\t"
    "v_max3_f32 v18, v18, v75, v88\n\t"
    "v_max3_f32 v18, v18, v89, v90\n\t"
    "v_max_f32 v18, v18, v91\n\t"
    "v_mov_b32 v19, v18\n\t"
    "s_nop 1\n\t"
    "v_permlane16_swap_b32 v18, v19\n\t"
    "s_nop 1\n\t"
    "v_max_f32 v18, v18, v19\n\t"
    "v_mov_b32 v19, v18\n\t"
    "s_nop 1\n\t"
    "v_permlane32_swap_b32 v18, v19\n\t"
    "s_nop 1\n\t"
    "v_max_f32 v18, v18, v19\n\t"
    "v_add_f32 v18, 0x40e00000, v18\n\t"
    "v_max_f32 v18, 0, v18\n\t"
    "v_mov_b32 v19, 0x43800000\n\t"
    "v_cmp_le_f32 vcc, v19, v152\n\t"
    "v_mov_b32 v19, 0x40c00000\n\t"
    "s_nop 1\n\t"
    "v_cndmask_b32 v19, 0, v19, vcc\n\t"
    "v_max_f32 v18, v18, v19\n\t"
    "v_exp_f32_e64 v20, -v18\n\t"
    "v_sub_f32 v72, v72, v18\n\t"
    "v_sub_f32 v73, v73, v18\n\t"
    "v_sub_f32 v74, v74, v18\n\t"
    "v_sub_f32 v75, v75, v18\n\t"
    "v_sub_f32 v88, v88, v18\n\t"
    "v_sub_f32 v89, v89, v18\n\t"
    "v_sub_f32 v90, v90, v18\n\t"
    "v_sub_f32 v91, v91, v18\n\t"
    "v_sub_f32 v40, v40, v18\n\t"
    "v_sub_f32 v41, v41, v18\n\t"
    "v_sub_f32 v42, v42, v18\n\t"
    "v_sub_f32 v43, v43, v18\n\t"
    "v_sub_f32 v56, v56, v18\n\t"
    "v_sub_f32 v57, v57, v18\n\t"
    "v_sub_f32 v58, v58, v18\n\t"
    "v_sub_f32 v59, v59, v18\n\t"
    "v_sub_f32 v136, v136, v18\n\t"
    "v_sub_f32 v137, v137, v18\n\t"
    "v_sub_f32 v138, v138, v18\n\t"
    "v_sub_f32 v139, v139, v18\n\t"
    "v_mul_f32 v152, v20, v152\n\t"
    "v_mul_f32 v153, v20, v153\n\t"
    "v_mul_f32 v154, v20, v154\n\t"
    "v_mul_f32 v155, v20, v155\n\t"
    "v_accvgpr_read_b32 v22, a8\n\t"
    "v_accvgpr_read_b32 v23, a9\n\t"
    "v_accvgpr_read_b32 v24, a10\n\t"
    "v_accvgpr_read_b32 v25, a11\n\t"
    "v_mul_f32 v22, v20, v22\n\t"
    "v_mul_f32 v23, v20, v23\n\t"
    "v_mul_f32 v24, v20, v24\n\t"
    "v_mul_f32 v25, v20, v25\n\t"
    "v_accvgpr_write_b32 a8, v22\n\t"
    "v_accvgpr_write_b32 a9, v23\n\t"
    "v_accvgpr_write_b32 a10, v24\n\t"
    "v_accvgpr_write_b32 a11, v25\n\t"
    "v_accvgpr_read_b32 v22, a24\n\t"
    "v_accvgpr_read_b32 v23, a25\n\t"
    "v_accvgpr_read_b32 v24, a26\n\t"
    "v_accvgpr_read_b32 v25, a27\n\t"
    "v_mul_f32 v22, v20, v22\n\t"
    "v_mul_f32 v23, v20, v23\n\t"
    "v_mul_f32 v24, v20, v24\n\t"
    "v_mul_f32 v25, v20, v25\n\t"
    "v_accvgpr_write_b32 a24, v22\n\t"
    "v_accvgpr_write_b32 a25, v23\n\t"
    "v_accvgpr_write_b32 a26, v24\n\t"
    "v_accvgpr_write_b32 a27, v25\n\t"
    "v_accvgpr_read_b32 v22, a40\n\t"
    "v_accvgpr_read_b32 v23, a41\n\t"
    "v_accvgpr_read_b32 v24, a42\n\t"
    "v_accvgpr_read_b32 v25, a43\n\t"
    "v_mul_f32 v22, v20, v22\n\t"
    "v_mul_f32 v23, v20, v23\n\t"
    "v_mul_f32 v24, v20, v24\n\t"
    "v_mul_f32 v25, v20, v25\n\t"
    "v_accvgpr_write_b32 a40, v22\n\t"
    "v_accvgpr_write_b32 a41, v23\n\t"
    "v_accvgpr_write_b32 a42, v24\n\t"
    "v_accvgpr_write_b32 a43, v25\n\t"
    "v_accvgpr_read_b32 v22, a56\n\t"
    "v_accvgpr_read_b32 v23, a57\n\t"
    "v_accvgpr_read_b32 v24, a58\n\t"
    "v_accvgpr_read_b32 v25, a59\n\t"
    "v_mul_f32 v22, v20, v22\n\t"
    "v_mul_f32 v23, v20, v23\n\t"
    "v_mul_f32 v24, v20, v24\n\t"
    "v_mul_f32 v25, v20, v25\n\t"
    "v_accvgpr_write_b32 a56, v22\n\t"
    "v_accvgpr_write_b32 a57, v23\n\t"
    "v_accvgpr_write_b32 a58, v24\n\t"
    "v_accvgpr_write_b32 a59, v25\n\t"
    "v_accvgpr_read_b32 v22, a72\n\t"
    "v_accvgpr_read_b32 v23, a73\n\t"
    "v_accvgpr_read_b32 v24, a74\n\t"
    "v_accvgpr_read_b32 v25, a75\n\t"
    "v_mul_f32 v22, v20, v22\n\t"
    "v_mul_f32 v23, v20, v23\n\t"
    "v_mul_f32 v24, v20, v24\n\t"
    "v_mul_f32 v25, v20, v25\n\t"
    "v_accvgpr_write_b32 a72, v22\n\t"
    "v_accvgpr_write_b32 a73, v23\n\t"
    "v_accvgpr_write_b32 a74, v24\n\t"
    "v_accvgpr_write_b32 a75, v25\n\t"
    "v_accvgpr_read_b32 v22, a88\n\t"
    "v_accvgpr_read_b32 v23, a89\n\t"
    "v_accvgpr_read_b32 v24, a90\n\t"
    "v_accvgpr_read_b32 v25, a91\n\t"
    "v_mul_f32 v22, v20, v22\n\t"
    "v_mul_f32 v23, v20, v23\n\t"
    "v_mul_f32 v24, v20, v24\n\t"
    "v_mul_f32 v25, v20, v25\n\t"
    "v_accvgpr_write_b32 a88, v22\n\t"
    "v_accvgpr_write_b32 a89, v23\n\t"
    "v_accvgpr_write_b32 a90, v24\n\t"
    "v_accvgpr_write_b32 a91, v25\n\t"
    "v_accvgpr_read_b32 v22, a104\n\t"
    "v_accvgpr_read_b32 v23, a105\n\t"
    "v_accvgpr_read_b32 v24, a106\n\t"
    "v_accvgpr_read_b32 v25, a107\n\t"
    "v_mul_f32 v22, v20, v22\n\t"
    "v_mul_f32 v23, v20, v23\n\t"
    "v_mul_f32 v24, v20, v24\n\t"
    "v_mul_f32 v25, v20, v25\n\t"
    "v_accvgpr_write_b32 a104, v22\n\t"
    "v_accvgpr_write_b32 a105, v23\n\t"
    "v_accvgpr_write_b32 a106, v24\n\t"
    "v_accvgpr_write_b32 a107, v25\n\t"
    "v_accvgpr_read_b32 v22, a120\n\t"
    "v_accvgpr_read_b32 v23, a121\n\t"
    "v_accvgpr_read_b32 v24, a122\n\t"
    "v_accvgpr_read_b32 v25, a123\n\t"
    "v_mul_f32 v22, v20, v22\n\t"
    "v_mul_f32 v23, v20, v23\n\t"
    "v_mul_f32 v24, v20, v24\n\t"
    "v_mul_f32 v25, v20, v25\n\t"
    "v_accvgpr_write_b32 a120, v22\n\t"
    "v_accvgpr_write_b32 a121, v23\n\t"
    "v_accvgpr_write_b32 a122, v24\n\t"
    "v_accvgpr_write_b32 a123, v25\n\t"
    "v_exp_f32 v22, v72\n\t"
    "v_exp_f32 v23, v73\n\t"
    "s_nop 0\n\t"
    "v_cvt_pk_bf16_f32 v120, v22, v23\n\t"
    "v_exp_f32 v22, v74\n\t"
    "v_exp_f32 v23, v75\n\t"
    "s_nop 0\n\t"
    "v_cvt_pk_bf16_f32 v121, v22, v23\n\t"
    "v_exp_f32 v22, v88\n\t"
    "v_exp_f32 v23, v89\n\t"
    "s_nop 0\n\t"
    "v_cvt_pk_bf16_f32 v122, v22, v23\n\t"
    "v_exp_f32 v22, v90\n\t"
    "v_exp_f32 v23, v91\n\t"
    "s_nop 0\n\t"
    "v_cvt_pk_bf16_f32 v123, v22, v23\n\t"
    "s_nop 1\n\t"
    "v_mfma_f32_16x16x32_bf16 v[168:171], v[176:179], v[120:123], v[152:155]\n\t"
    "v_max3_f32 v18, v76, v77, v78\n\t"
    "v_max3_f32 v18, v18, v79, v92\n\t"
    "v_max3_f32 v18, v18, v93, v94\n\t"
    "v_max_f32 v18, v18, v95\n\t"
    "v_mov_b32 v19, v18\n\t"
    "s_nop 1\n\t"
    "v_permlane16_swap_b32 v18, v19\n\t"
    "s_nop 1\n\t"
    "v_max_f32 v18, v18, v19\n\t"
    "v_mov_b32 v19, v18\n\t"
    "s_nop 1\n\t"
    "v_permlane32_swap_b32 v18, v19\n\t"
    "s_nop 1\n\t"
    "v_max_f32 v18, v18, v19\n\t"
    "v_add_f32 v18, 0x40e00000, v18\n\t"
    "v_max_f32 v18, 0, v18\n\t"
    "v_mov_b32 v19, 0x43800000\n\t"
    "v_cmp_le_f32 vcc, v19, v156\n\t"
    "v_mov_b32 v19, 0x40c00000\n\t"
    "s_nop 1\n\t"
    "v_cndmask_b32 v19, 0, v19, vcc\n\t"
    "v_max_f32 v18, v18, v19\n\t"
    "v_exp_f32_e64 v20, -v18\n\t"
    "v_sub_f32 v76, v76, v18\n\t"
    "v_sub_f32 v77, v77, v18\n\t"
    "v_sub_f32 v78, v78, v18\n\t"
    "v_sub_f32 v79, v79, v18\n\t"
    "v_sub_f32 v92, v92, v18\n\t"
    "v_sub_f32 v93, v93, v18\n\t"
    "v_sub_f32 v94, v94, v18\n\t"
    "v_sub_f32 v95, v95, v18\n\t"
    "v_sub_f32 v44, v44, v18\n\t"
    "v_sub_f32 v45, v45, v18\n\t"
    "v_sub_f32 v46, v46, v18\n\t"
    "v_sub_f32 v47, v47, v18\n\t"
    "v_sub_f32 v60, v60, v18\n\t"
    "v_sub_f32 v61, v61, v18\n\t"
    "v_sub_f32 v62, v62, v18\n\t"
    "v_sub_f32 v63, v63, v18\n\t"
    "v_sub_f32 v140, v140, v18\n\t"
    "v_sub_f32 v141, v141, v18\n\t"
    "v_sub_f32 v142, v142, v18\n\t"
    "v_sub_f32 v143, v143, v18\n\t"
    "v_mul_f32 v156, v20, v156\n\t"
    "v_mul_f32 v157, v20, v157\n\t"
    "v_mul_f32 v158, v20, v158\n\t"
    "v_mul_f32 v159, v20, v159\n\t"
    "v_accvgpr_read_b32 v22, a12\n\t"
    "v_accvgpr_read_b32 v23, a13\n\t"
    "v_accvgpr_read_b32 v24, a14\n\t"
    "v_accvgpr_read_b32 v25, a15\n\t"
    "v_mul_f32 v22, v20, v22\n\t"
    "v_mul_f32 v23, v20, v23\n\t"
    "v_mul_f32 v24, v20, v24\n\t"
    "v_mul_f32 v25, v20, v25\n\t"
    "v_accvgpr_write_b32 a12, v22\n\t"
    "v_accvgpr_write_b32 a13, v23\n\t"
    "v_accvgpr_write_b32 a14, v24\n\t"
    "v_accvgpr_write_b32 a15, v25\n\t"
    "v_accvgpr_read_b32 v22, a28\n\t"
    "v_accvgpr_read_b32 v23, a29\n\t"
    "v_accvgpr_read_b32 v24, a30\n\t"
    "v_accvgpr_read_b32 v25, a31\n\t"
    "v_mul_f32 v22, v20, v22\n\t"
    "v_mul_f32 v23, v20, v23\n\t"
    "v_mul_f32 v24, v20, v24\n\t"
    "v_mul_f32 v25, v20, v25\n\t"
    "v_accvgpr_write_b32 a28, v22\n\t"
    "v_accvgpr_write_b32 a29, v23\n\t"
    "v_accvgpr_write_b32 a30, v24\n\t"
    "v_accvgpr_write_b32 a31, v25\n\t"
    "v_accvgpr_read_b32 v22, a44\n\t"
    "v_accvgpr_read_b32 v23, a45\n\t"
    "v_accvgpr_read_b32 v24, a46\n\t"
    "v_accvgpr_read_b32 v25, a47\n\t"
    "v_mul_f32 v22, v20, v22\n\t"
    "v_mul_f32 v23, v20, v23\n\t"
    "v_mul_f32 v24, v20, v24\n\t"
    "v_mul_f32 v25, v20, v25\n\t"
    "v_accvgpr_write_b32 a44, v22\n\t"
    "v_accvgpr_write_b32 a45, v23\n\t"
    "v_accvgpr_write_b32 a46, v24\n\t"
    "v_accvgpr_write_b32 a47, v25\n\t"
    "v_accvgpr_read_b32 v22, a60\n\t"
    "v_accvgpr_read_b32 v23, a61\n\t"
    "v_accvgpr_read_b32 v24, a62\n\t"
    "v_accvgpr_read_b32 v25, a63\n\t"
    "v_mul_f32 v22, v20, v22\n\t"
    "v_mul_f32 v23, v20, v23\n\t"
    "v_mul_f32 v24, v20, v24\n\t"
    "v_mul_f32 v25, v20, v25\n\t"
    "v_accvgpr_write_b32 a60, v22\n\t"
    "v_accvgpr_write_b32 a61, v23\n\t"
    "v_accvgpr_write_b32 a62, v24\n\t"
    "v_accvgpr_write_b32 a63, v25\n\t"
    "v_accvgpr_read_b32 v22, a76\n\t"
    "v_accvgpr_read_b32 v23, a77\n\t"
    "v_accvgpr_read_b32 v24, a78\n\t"
    "v_accvgpr_read_b32 v25, a79\n\t"
    "v_mul_f32 v22, v20, v22\n\t"
    "v_mul_f32 v23, v20, v23\n\t"
    "v_mul_f32 v24, v20, v24\n\t"
    "v_mul_f32 v25, v20, v25\n\t"
    "v_accvgpr_write_b32 a76, v22\n\t"
    "v_accvgpr_write_b32 a77, v23\n\t"
    "v_accvgpr_write_b32 a78, v24\n\t"
    "v_accvgpr_write_b32 a79, v25\n\t"
    "v_accvgpr_read_b32 v22, a92\n\t"
    "v_accvgpr_read_b32 v23, a93\n\t"
    "v_accvgpr_read_b32 v24, a94\n\t"
    "v_accvgpr_read_b32 v25, a95\n\t"
    "v_mul_f32 v22, v20, v22\n\t"
    "v_mul_f32 v23, v20, v23\n\t"
    "v_mul_f32 v24, v20, v24\n\t"
    "v_mul_f32 v25, v20, v25\n\t"
    "v_accvgpr_write_b32 a92, v22\n\t"
    "v_accvgpr_write_b32 a93, v23\n\t"
    "v_accvgpr_write_b32 a94, v24\n\t"
    "v_accvgpr_write_b32 a95, v25\n\t"
    "v_accvgpr_read_b32 v22, a108\n\t"
    "v_accvgpr_read_b32 v23, a109\n\t"
    "v_accvgpr_read_b32 v24, a110\n\t"
    "v_accvgpr_read_b32 v25, a111\n\t"
    "v_mul_f32 v22, v20, v22\n\t"
    "v_mul_f32 v23, v20, v23\n\t"
    "v_mul_f32 v24, v20, v24\n\t"
    "v_mul_f32 v25, v20, v25\n\t"
    "v_accvgpr_write_b32 a108, v22\n\t"
    "v_accvgpr_write_b32 a109, v23\n\t"
    "v_accvgpr_write_b32 a110, v24\n\t"
    "v_accvgpr_write_b32 a111, v25\n\t"
    "v_accvgpr_read_b32 v22, a124\n\t"
    "v_accvgpr_read_b32 v23, a125\n\t"
    "v_accvgpr_read_b32 v24, a126\n\t"
    "v_accvgpr_read_b32 v25, a127\n\t"
    "v_mul_f32 v22, v20, v22\n\t"
    "v_mul_f32 v23, v20, v23\n\t"
    "v_mul_f32 v24, v20, v24\n\t"
    "v_mul_f32 v25, v20, v25\n\t"
    "v_accvgpr_write_b32 a124, v22\n\t"
    "v_accvgpr_write_b32 a125, v23\n\t"
    "v_accvgpr_write_b32 a126, v24\n\t"
    "v_accvgpr_write_b32 a127, v25\n\t"
    "v_exp_f32 v22, v76\n\t"
    "v_exp_f32 v23, v77\n\t"
    "s_nop 0\n\t"
    "v_cvt_pk_bf16_f32 v124, v22, v23\n\t"
    "v_exp_f32 v22, v78\n\t"
    "v_exp_f32 v23, v79\n\t"
    "s_nop 0\n\t"
    "v_cvt_pk_bf16_f32 v125, v22, v23\n\t"
    "v_exp_f32 v22, v92\n\t"
    "v_exp_f32 v23, v93\n\t"
    "s_nop 0\n\t"
    "v_cvt_pk_bf16_f32 v126, v22, v23\n\t"
    "v_exp_f32 v22, v94\n\t"
    "v_exp_f32 v23, v95\n\t"
    "s_nop 0\n\t"
    "v_cvt_pk_bf16_f32 v127, v22, v23\n\t"
    "s_nop 1\n\t"
    "v_mfma_f32_16x16x32_bf16 v[172:175], v[176:179], v[124:127], v[156:159]\n\t"
    "s_nop 7\n\t"
    "s_branch L_ret1_%=\n\t"
    "L_rare0sub_%=:\n\t"
    "s_nop 15\n\t"
    "v_max3_f32 v18, v32, v33, v34\n\t"
    "v_max3_f32 v18, v18, v35, v48\n\t"
    "v_max3_f32 v18, v18, v49, v50\n\t"
    "v_max_f32 v18, v18, v51\n\t"
    "v_mov_b32 v19, v18\n\t"
    "s_nop 1\n\t"
    "v_permlane16_swap_b32 v18, v19\n\t"
    "s_nop 1\n\t"
    "v_max_f32 v18, v18, v19\n\t"
    "v_mov_b32 v19, v18\n\t"
    "s_nop 1\n\t"
    "v_permlane32_swap_b32 v18, v19\n\t"
    "s_nop 1\n\t"
    "v_max_f32 v18, v18, v19\n\t"
    "v_add_f32 v18, 0x40e00000, v18\n\t"
    "v_max_f32 v18, 0, v18\n\t"
    "v_mov_b32 v19, 0x43800000\n\t"
    "v_cmp_le_f32 vcc, v19, v160\n\t"
    "v_mov_b32 v19, 0x40c00000\n\t"
    "s_nop 1\n\t"
    "v_cndmask_b32 v19, 0, v19, vcc\n\t"
    "v_max_f32 v18, v18, v19\n\t"
    "v_exp_f32_e64 v20, -v18\n\t"
    "v_sub_f32 v32, v32, v18\n\t"
    "v_sub_f32 v33, v33, v18\n\t"
    "v_sub_f32 v34, v34, v18\n\t"
    "v_sub_f32 v35, v35, v18\n\t"
    "v_sub_f32 v48, v48, v18\n\t"
    "v_sub_f32 v49, v49, v18\n\t"
    "v_sub_f32 v50, v50, v18\n\t"
    "v_sub_f32 v51, v51, v18\n\t"
    "v_sub_f32 v64, v64, v18\n\t"
    "v_sub_f32 v65, v65, v18\n\t"
    "v_sub_f32 v66, v66, v18\n\t"
    "v_sub_f32 v67, v67, v18\n\t"
    "v_sub_f32 v80, v80, v18\n\t"
    "v_sub_f32 v81, v81, v18\n\t"
    "v_sub_f32 v82, v82, v18\n\t"
    "v_sub_f32 v83, v83, v18\n\t"
    "v_sub_f32 v128, v128, v18\n\t"
    "v_sub_f32 v129, v129, v18\n\t"
    "v_sub_f32 v130, v130, v18\n\t"
    "v_sub_f32 v131, v131, v18\n\t"
    "v_mul_f32 v160, v20, v160\n\t"
    "v_mul_f32 v161, v20, v161\n\t"
    "v_mul_f32 v162, v20, v162\n\t"
    "v_mul_f32 v163, v20, v163\n\t"
    "v_accvgpr_read_b32 v22, a0\n\t"
    "v_accvgpr_read_b32 v23, a1\n\t"
    "v_accvgpr_read_b32 v24, a2\n\t"
    "v_accvgpr_read_b32 v25, a3\n\t"
    "v_mul_f32 v22, v20, v22\n\t"
    "v_mul_f32 v23, v20, v23\n\t"
    "v_mul_f32 v24, v20, v24\n\t"
    "v_mul_f32 v25, v20, v25\n\t"
    "v_accvgpr_write_b32 a0, v22\n\t"
    "v_accvgpr_write_b32 a1, v23\n\t"
    "v_accvgpr_write_b32 a2, v24\n\t"
    "v_accvgpr_write_b32 a3, v25\n\t"
    "v_accvgpr_read_b32 v22, a16\n\t"
    "v_accvgpr_read_b32 v23, a17\n\t"
    "v_accvgpr_read_b32 v24, a18\n\t"
    "v_accvgpr_read_b32 v25, a19\n\t"
    "v_mul_f32 v22, v20, v22\n\t"
    "v_mul_f32 v23, v20, v23\n\t"
    "v_mul_f32 v24, v20, v24\n\t"
    "v_mul_f32 v25, v20, v25\n\t"
    "v_accvgpr_write_b32 a16, v22\n\t"
    "v_accvgpr_write_b32 a17, v23\n\t"
    "v_accvgpr_write_b32 a18, v24\n\t"
    "v_accvgpr_write_b32 a19, v25\n\t"
    "v_accvgpr_read_b32 v22, a32\n\t"
    "v_accvgpr_read_b32 v23, a33\n\t"
    "v_accvgpr_read_b32 v24, a34\n\t"
    "v_accvgpr_read_b32 v25, a35\n\t"
    "v_mul_f32 v22, v20, v22\n\t"
    "v_mul_f32 v23, v20, v23\n\t"
    "v_mul_f32 v24, v20, v24\n\t"
    "v_mul_f32 v25, v20, v25\n\t"
    "v_accvgpr_write_b32 a32, v22\n\t"
    "v_accvgpr_write_b32 a33, v23\n\t"
    "v_accvgpr_write_b32 a34, v24\n\t"
    "v_accvgpr_write_b32 a35, v25\n\t"
    "v_accvgpr_read_b32 v22, a48\n\t"
    "v_accvgpr_read_b32 v23, a49\n\t"
    "v_accvgpr_read_b32 v24, a50\n\t"
    "v_accvgpr_read_b32 v25, a51\n\t"
    "v_mul_f32 v22, v20, v22\n\t"
    "v_mul_f32 v23, v20, v23\n\t"
    "v_mul_f32 v24, v20, v24\n\t"
    "v_mul_f32 v25, v20, v25\n\t"
    "v_accvgpr_write_b32 a48, v22\n\t"
    "v_accvgpr_write_b32 a49, v23\n\t"
    "v_accvgpr_write_b32 a50, v24\n\t"
    "v_accvgpr_write_b32 a51, v25\n\t"
    "v_accvgpr_read_b32 v22, a64\n\t"
    "v_accvgpr_read_b32 v23, a65\n\t"
    "v_accvgpr_read_b32 v24, a66\n\t"
    "v_accvgpr_read_b32 v25, a67\n\t"
    "v_mul_f32 v22, v20, v22\n\t"
    "v_mul_f32 v23, v20, v23\n\t"
    "v_mul_f32 v24, v20, v24\n\t"
    "v_mul_f32 v25, v20, v25\n\t"
    "v_accvgpr_write_b32 a64, v22\n\t"
    "v_accvgpr_write_b32 a65, v23\n\t"
    "v_accvgpr_write_b32 a66, v24\n\t"
    "v_accvgpr_write_b32 a67, v25\n\t"
    "v_accvgpr_read_b32 v22, a80\n\t"
    "v_accvgpr_read_b32 v23, a81\n\t"
    "v_accvgpr_read_b32 v24, a82\n\t"
    "v_accvgpr_read_b32 v25, a83\n\t"
    "v_mul_f32 v22, v20, v22\n\t"
    "v_mul_f32 v23, v20, v23\n\t"
    "v_mul_f32 v24, v20, v24\n\t"
    "v_mul_f32 v25, v20, v25\n\t"
    "v_accvgpr_write_b32 a80, v22\n\t"
    "v_accvgpr_write_b32 a81, v23\n\t"
    "v_accvgpr_write_b32 a82, v24\n\t"
    "v_accvgpr_write_b32 a83, v25\n\t"
    "v_accvgpr_read_b32 v22, a96\n\t"
    "v_accvgpr_read_b32 v23, a97\n\t"
    "v_accvgpr_read_b32 v24, a98\n\t"
    "v_accvgpr_read_b32 v25, a99\n\t"
    "v_mul_f32 v22, v20, v22\n\t"
    "v_mul_f32 v23, v20, v23\n\t"
    "v_mul_f32 v24, v20, v24\n\t"
    "v_mul_f32 v25, v20, v25\n\t"
    "v_accvgpr_write_b32 a96, v22\n\t"
    "v_accvgpr_write_b32 a97, v23\n\t"
    "v_accvgpr_write_b32 a98, v24\n\t"
    "v_accvgpr_write_b32 a99, v25\n\t"
    "v_accvgpr_read_b32 v22, a112\n\t"
    "v_accvgpr_read_b32 v23, a113\n\t"
    "v_accvgpr_read_b32 v24, a114\n\t"
    "v_accvgpr_read_b32 v25, a115\n\t"
    "v_mul_f32 v22, v20, v22\n\t"
    "v_mul_f32 v23, v20, v23\n\t"
    "v_mul_f32 v24, v20, v24\n\t"
    "v_mul_f32 v25, v20, v25\n\t"
    "v_accvgpr_write_b32 a112, v22\n\t"
    "v_accvgpr_write_b32 a113, v23\n\t"
    "v_accvgpr_write_b32 a114, v24\n\t"
    "v_accvgpr_write_b32 a115, v25\n\t"
    "v_exp_f32 v22, v32\n\t"
    "v_exp_f32 v23, v33\n\t"
    "s_nop 0\n\t"
    "v_cvt_pk_bf16_f32 v96, v22, v23\n\t"
    "v_exp_f32 v22, v34\n\t"
    "v_exp_f32 v23, v35\n\t"
    "s_nop 0\n\t"
    "v_cvt_pk_bf16_f32 v97, v22, v23\n\t"
    "v_exp_f32 v22, v48\n\t"
    "v_exp_f32 v23, v49\n\t"
    "s_nop 0\n\t"
    "v_cvt_pk_bf16_f32 v98, v22, v23\n\t"
    "v_exp_f32 v22, v50\n\t"
    "v_exp_f32 v23, v51\n\t"
    "s_nop 0\n\t"
    "v_cvt_pk_bf16_f32 v99, v22, v23\n\t"
    "s_nop 1\n\t"
    "v_mfma_f32_16x16x32_bf16 v[144:147], v[176:179], v[96:99], v[160:163]\n\t"
    "v_max3_f32 v18, v36, v37, v38\n\t"
    "v_max3_f32 v18, v18, v39, v52\n\t"
    "v_max3_f32 v18, v18, v53, v54\n\t"
    "v_max_f32 v18, v18, v55\n\t"
    "v_mov_b32 v19, v18\n\t"
    "s_nop 1\n\t"
    "v_permlane16_swap_b32 v18, v19\n\t"
    "s_nop 1\n\t"
    "v_max_f32 v18, v18, v19\n\t"
    "v_mov_b32 v19, v18\n\t"
    "s_nop 1\n\t"
    "v_permlane32_swap_b32 v18, v19\n\t"
    "s_nop 1\n\t"
    "v_max_f32 v18, v18, v19\n\t"
    "v_add_f32 v18, 0x40e00000, v18\n\t"
    "v_max_f32 v18, 0, v18\n\t"
    "v_mov_b32 v19, 0x43800000\n\t"
    "v_cmp_le_f32 vcc, v19, v164\n\t"
    "v_mov_b32 v19, 0x40c00000\n\t"
    "s_nop 1\n\t"
    "v_cndmask_b32 v19, 0, v19, vcc\n\t"
    "v_max_f32 v18, v18, v19\n\t"
    "v_exp_f32_e64 v20, -v18\n\t"
    "v_sub_f32 v36, v36, v18\n\t"
    "v_sub_f32 v37, v37, v18\n\t"
    "v_sub_f32 v38, v38, v18\n\t"
    "v_sub_f32 v39, v39, v18\n\t"
    "v_sub_f32 v52, v52, v18\n\t"
    "v_sub_f32 v53, v53, v18\n\t"
    "v_sub_f32 v54, v54, v18\n\t"
    "v_sub_f32 v55, v55, v18\n\t"
    "v_sub_f32 v68, v68, v18\n\t"
    "v_sub_f32 v69, v69, v18\n\t"
    "v_sub_f32 v70, v70, v18\n\t"
    "v_sub_f32 v71, v71, v18\n\t"
    "v_sub_f32 v84, v84, v18\n\t"
    "v_sub_f32 v85, v85, v18\n\t"
    "v_sub_f32 v86, v86, v18\n\t"
    "v_sub_f32 v87, v87, v18\n\t"
    "v_sub_f32 v132, v132, v18\n\t"
    "v_sub_f32 v133, v133, v18\n\t"
    "v_sub_f32 v134, v134, v18\n\t"
    "v_sub_f32 v135, v135, v18\n\t"
    "v_mul_f32 v164, v20, v164\n\t"
    "v_mul_f32 v165, v20, v165\n\t"
    "v_mul_f32 v166, v20, v166\n\t"
    "v_mul_f32 v167, v20, v167\n\t"
    "v_accvgpr_read_b32 v22, a4\n\t"
    "v_accvgpr_read_b32 v23, a5\n\t"
    "v_accvgpr_read_b32 v24, a6\n\t"
    "v_accvgpr_read_b32 v25, a7\n\t"
    "v_mul_f32 v22, v20, v22\n\t"
    "v_mul_f32 v23, v20, v23\n\t"
    "v_mul_f32 v24, v20, v24\n\t"
    "v_mul_f32 v25, v20, v25\n\t"
    "v_accvgpr_write_b32 a4, v22\n\t"
    "v_accvgpr_write_b32 a5, v23\n\t"
    "v_accvgpr_write_b32 a6, v24\n\t"
    "v_accvgpr_write_b32 a7, v25\n\t"
    "v_accvgpr_read_b32 v22, a20\n\t"
    "v_accvgpr_read_b32 v23, a21\n\t"
    "v_accvgpr_read_b32 v24, a22\n\t"
    "v_accvgpr_read_b32 v25, a23\n\t"
    "v_mul_f32 v22, v20, v22\n\t"
    "v_mul_f32 v23, v20, v23\n\t"
    "v_mul_f32 v24, v20, v24\n\t"
    "v_mul_f32 v25, v20, v25\n\t"
    "v_accvgpr_write_b32 a20, v22\n\t"
    "v_accvgpr_write_b32 a21, v23\n\t"
    "v_accvgpr_write_b32 a22, v24\n\t"
    "v_accvgpr_write_b32 a23, v25\n\t"
    "v_accvgpr_read_b32 v22, a36\n\t"
    "v_accvgpr_read_b32 v23, a37\n\t"
    "v_accvgpr_read_b32 v24, a38\n\t"
    "v_accvgpr_read_b32 v25, a39\n\t"
    "v_mul_f32 v22, v20, v22\n\t"
    "v_mul_f32 v23, v20, v23\n\t"
    "v_mul_f32 v24, v20, v24\n\t"
    "v_mul_f32 v25, v20, v25\n\t"
    "v_accvgpr_write_b32 a36, v22\n\t"
    "v_accvgpr_write_b32 a37, v23\n\t"
    "v_accvgpr_write_b32 a38, v24\n\t"
    "v_accvgpr_write_b32 a39, v25\n\t"
    "v_accvgpr_read_b32 v22, a52\n\t"
    "v_accvgpr_read_b32 v23, a53\n\t"
    "v_accvgpr_read_b32 v24, a54\n\t"
    "v_accvgpr_read_b32 v25, a55\n\t"
    "v_mul_f32 v22, v20, v22\n\t"
    "v_mul_f32 v23, v20, v23\n\t"
    "v_mul_f32 v24, v20, v24\n\t"
    "v_mul_f32 v25, v20, v25\n\t"
    "v_accvgpr_write_b32 a52, v22\n\t"
    "v_accvgpr_write_b32 a53, v23\n\t"
    "v_accvgpr_write_b32 a54, v24\n\t"
    "v_accvgpr_write_b32 a55, v25\n\t"
    "v_accvgpr_read_b32 v22, a68\n\t"
    "v_accvgpr_read_b32 v23, a69\n\t"
    "v_accvgpr_read_b32 v24, a70\n\t"
    "v_accvgpr_read_b32 v25, a71\n\t"
    "v_mul_f32 v22, v20, v22\n\t"
    "v_mul_f32 v23, v20, v23\n\t"
    "v_mul_f32 v24, v20, v24\n\t"
    "v_mul_f32 v25, v20, v25\n\t"
    "v_accvgpr_write_b32 a68, v22\n\t"
    "v_accvgpr_write_b32 a69, v23\n\t"
    "v_accvgpr_write_b32 a70, v24\n\t"
    "v_accvgpr_write_b32 a71, v25\n\t"
    "v_accvgpr_read_b32 v22, a84\n\t"
    "v_accvgpr_read_b32 v23, a85\n\t"
    "v_accvgpr_read_b32 v24, a86\n\t"
    "v_accvgpr_read_b32 v25, a87\n\t"
    "v_mul_f32 v22, v20, v22\n\t"
    "v_mul_f32 v23, v20, v23\n\t"
    "v_mul_f32 v24, v20, v24\n\t"
    "v_mul_f32 v25, v20, v25\n\t"
    "v_accvgpr_write_b32 a84, v22\n\t"
    "v_accvgpr_write_b32 a85, v23\n\t"
    "v_accvgpr_write_b32 a86, v24\n\t"
    "v_accvgpr_write_b32 a87, v25\n\t"
    "v_accvgpr_read_b32 v22, a100\n\t"
    "v_accvgpr_read_b32 v23, a101\n\t"
    "v_accvgpr_read_b32 v24, a102\n\t"
    "v_accvgpr_read_b32 v25, a103\n\t"
    "v_mul_f32 v22, v20, v22\n\t"
    "v_mul_f32 v23, v20, v23\n\t"
    "v_mul_f32 v24, v20, v24\n\t"
    "v_mul_f32 v25, v20, v25\n\t"
    "v_accvgpr_write_b32 a100, v22\n\t"
    "v_accvgpr_write_b32 a101, v23\n\t"
    "v_accvgpr_write_b32 a102, v24\n\t"
    "v_accvgpr_write_b32 a103, v25\n\t"
    "v_accvgpr_read_b32 v22, a116\n\t"
    "v_accvgpr_read_b32 v23, a117\n\t"
    "v_accvgpr_read_b32 v24, a118\n\t"
    "v_accvgpr_read_b32 v25, a119\n\t"
    "v_mul_f32 v22, v20, v22\n\t"
    "v_mul_f32 v23, v20, v23\n\t"
    "v_mul_f32 v24, v20, v24\n\t"
    "v_mul_f32 v25, v20, v25\n\t"
    "v_accvgpr_write_b32 a116, v22\n\t"
    "v_accvgpr_write_b32 a117, v23\n\t"
    "v_accvgpr_write_b32 a118, v24\n\t"
    "v_accvgpr_write_b32 a119, v25\n\t"
    "v_exp_f32 v22, v36\n\t"
    "v_exp_f32 v23, v37\n\t"
    "s_nop 0\n\t"
    "v_cvt_pk_bf16_f32 v100, v22, v23\n\t"
    "v_exp_f32 v22, v38\n\t"
    "v_exp_f32 v23, v39\n\t"
    "s_nop 0\n\t"
    "v_cvt_pk_bf16_f32 v101, v22, v23\n\t"
    "v_exp_f32 v22, v52\n\t"
    "v_exp_f32 v23, v53\n\t"
    "s_nop 0\n\t"
    "v_cvt_pk_bf16_f32 v102, v22, v23\n\t"
    "v_exp_f32 v22, v54\n\t"
    "v_exp_f32 v23, v55\n\t"
    "s_nop 0\n\t"
    "v_cvt_pk_bf16_f32 v103, v22, v23\n\t"
    "s_nop 1\n\t"
    "v_mfma_f32_16x16x32_bf16 v[148:151], v[176:179], v[100:103], v[164:167]\n\t"
    "v_max3_f32 v18, v40, v41, v42\n\t"
    "v_max3_f32 v18, v18, v43, v56\n\t"
    "v_max3_f32 v18, v18, v57, v58\n\t"
    "v_max_f32 v18, v18, v59\n\t"
    "v_mov_b32 v19, v18\n\t"
    "s_nop 1\n\t"
    "v_permlane16_swap_b32 v18, v19\n\t"
    "s_nop 1\n\t"
    "v_max_f32 v18, v18, v19\n\t"
    "v_mov_b32 v19, v18\n\t"
    "s_nop 1\n\t"
    "v_permlane32_swap_b32 v18, v19\n\t"
    "s_nop 1\n\t"
    "v_max_f32 v18, v18, v19\n\t"
    "v_add_f32 v18, 0x40e00000, v18\n\t"
    "v_max_f32 v18, 0, v18\n\t"
    "v_mov_b32 v19, 0x43800000\n\t"
    "v_cmp_le_f32 vcc, v19, v168\n\t"
    "v_mov_b32 v19, 0x40c00000\n\t"
    "s_nop 1\n\t"
    "v_cndmask_b32 v19, 0, v19, vcc\n\t"
    "v_max_f32 v18, v18, v19\n\t"
    "v_exp_f32_e64 v20, -v18\n\t"
    "v_sub_f32 v40, v40, v18\n\t"
    "v_sub_f32 v41, v41, v18\n\t"
    "v_sub_f32 v42, v42, v18\n\t"
    "v_sub_f32 v43, v43, v18\n\t"
    "v_sub_f32 v56, v56, v18\n\t"
    "v_sub_f32 v57, v57, v18\n\t"
    "v_sub_f32 v58, v58, v18\n\t"
    "v_sub_f32 v59, v59, v18\n\t"
    "v_sub_f32 v72, v72, v18\n\t"
    "v_sub_f32 v73, v73, v18\n\t"
    "v_sub_f32 v74, v74, v18\n\t"
    "v_sub_f32 v75, v75, v18\n\t"
    "v_sub_f32 v88, v88, v18\n\t"
    "v_sub_f32 v89, v89, v18\n\t"
    "v_sub_f32 v90, v90, v18\n\t"
    "v_sub_f32 v91, v91, v18\n\t"
    "v_sub_f32 v136, v136, v18\n\t"
    "v_sub_f32 v137, v137, v18\n\t"
    "v_sub_f32 v138, v138, v18\n\t"
    "v_sub_f32 v139, v139, v18\n\t"
    "v_mul_f32 v168, v20, v168\n\t"
    "v_mul_f32 v169, v20, v169\n\t"
    "v_mul_f32 v170, v20, v170\n\t"
    "v_mul_f32 v171, v20, v171\n\t"
    "v_accvgpr_read_b32 v22, a8\n\t"
    "v_accvgpr_read_b32 v23, a9\n\t"
    "v_accvgpr_read_b32 v24, a10\n\t"
    "v_accvgpr_read_b32 v25, a11\n\t"
    "v_mul_f32 v22, v20, v22\n\t"
    "v_mul_f32 v23, v20, v23\n\t"
    "v_mul_f32 v24, v20, v24\n\t"
    "v_mul_f32 v25, v20, v25\n\t"
    "v_accvgpr_write_b32 a8, v22\n\t"
    "v_accvgpr_write_b32 a9, v23\n\t"
    "v_accvgpr_write_b32 a10, v24\n\t"
    "v_accvgpr_write_b32 a11, v25\n\t"
    "v_accvgpr_read_b32 v22, a24\n\t"
    "v_accvgpr_read_b32 v23, a25\n\t"
    "v_accvgpr_read_b32 v24, a26\n\t"
    "v_accvgpr_read_b32 v25, a27\n\t"
    "v_mul_f32 v22, v20, v22\n\t"
    "v_mul_f32 v23, v20, v23\n\t"
    "v_mul_f32 v24, v20, v24\n\t"
    "v_mul_f32 v25, v20, v25\n\t"
    "v_accvgpr_write_b32 a24, v22\n\t"
    "v_accvgpr_write_b32 a25, v23\n\t"
    "v_accvgpr_write_b32 a26, v24\n\t"
    "v_accvgpr_write_b32 a27, v25\n\t"
    "v_accvgpr_read_b32 v22, a40\n\t"
    "v_accvgpr_read_b32 v23, a41\n\t"
    "v_accvgpr_read_b32 v24, a42\n\t"
    "v_accvgpr_read_b32 v25, a43\n\t"
    "v_mul_f32 v22, v20, v22\n\t"
    "v_mul_f32 v23, v20, v23\n\t"
    "v_mul_f32 v24, v20, v24\n\t"
    "v_mul_f32 v25, v20, v25\n\t"
    "v_accvgpr_write_b32 a40, v22\n\t"
    "v_accvgpr_write_b32 a41, v23\n\t"
    "v_accvgpr_write_b32 a42, v24\n\t"
    "v_accvgpr_write_b32 a43, v25\n\t"
    "v_accvgpr_read_b32 v22, a56\n\t"
    "v_accvgpr_read_b32 v23, a57\n\t"
    "v_accvgpr_read_b32 v24, a58\n\t"
    "v_accvgpr_read_b32 v25, a59\n\t"
    "v_mul_f32 v22, v20, v22\n\t"
    "v_mul_f32 v23, v20, v23\n\t"
    "v_mul_f32 v24, v20, v24\n\t"
    "v_mul_f32 v25, v20, v25\n\t"
    "v_accvgpr_write_b32 a56, v22\n\t"
    "v_accvgpr_write_b32 a57, v23\n\t"
    "v_accvgpr_write_b32 a58, v24\n\t"
    "v_accvgpr_write_b32 a59, v25\n\t"
    "v_accvgpr_read_b32 v22, a72\n\t"
    "v_accvgpr_read_b32 v23, a73\n\t"
    "v_accvgpr_read_b32 v24, a74\n\t"
    "v_accvgpr_read_b32 v25, a75\n\t"
    "v_mul_f32 v22, v20, v22\n\t"
    "v_mul_f32 v23, v20, v23\n\t"
    "v_mul_f32 v24, v20, v24\n\t"
    "v_mul_f32 v25, v20, v25\n\t"
    "v_accvgpr_write_b32 a72, v22\n\t"
    "v_accvgpr_write_b32 a73, v23\n\t"
    "v_accvgpr_write_b32 a74, v24\n\t"
    "v_accvgpr_write_b32 a75, v25\n\t"
    "v_accvgpr_read_b32 v22, a88\n\t"
    "v_accvgpr_read_b32 v23, a89\n\t"
    "v_accvgpr_read_b32 v24, a90\n\t"
    "v_accvgpr_read_b32 v25, a91\n\t"
    "v_mul_f32 v22, v20, v22\n\t"
    "v_mul_f32 v23, v20, v23\n\t"
    "v_mul_f32 v24, v20, v24\n\t"
    "v_mul_f32 v25, v20, v25\n\t"
    "v_accvgpr_write_b32 a88, v22\n\t"
    "v_accvgpr_write_b32 a89, v23\n\t"
    "v_accvgpr_write_b32 a90, v24\n\t"
    "v_accvgpr_write_b32 a91, v25\n\t"
    "v_accvgpr_read_b32 v22, a104\n\t"
    "v_accvgpr_read_b32 v23, a105\n\t"
    "v_accvgpr_read_b32 v24, a106\n\t"
    "v_accvgpr_read_b32 v25, a107\n\t"
    "v_mul_f32 v22, v20, v22\n\t"
    "v_mul_f32 v23, v20, v23\n\t"
    "v_mul_f32 v24, v20, v24\n\t"
    "v_mul_f32 v25, v20, v25\n\t"
    "v_accvgpr_write_b32 a104, v22\n\t"
    "v_accvgpr_write_b32 a105, v23\n\t"
    "v_accvgpr_write_b32 a106, v24\n\t"
    "v_accvgpr_write_b32 a107, v25\n\t"
    "v_accvgpr_read_b32 v22, a120\n\t"
    "v_accvgpr_read_b32 v23, a121\n\t"
    "v_accvgpr_read_b32 v24, a122\n\t"
    "v_accvgpr_read_b32 v25, a123\n\t"
    "v_mul_f32 v22, v20, v22\n\t"
    "v_mul_f32 v23, v20, v23\n\t"
    "v_mul_f32 v24, v20, v24\n\t"
    "v_mul_f32 v25, v20, v25\n\t"
    "v_accvgpr_write_b32 a120, v22\n\t"
    "v_accvgpr_write_b32 a121, v23\n\t"
    "v_accvgpr_write_b32 a122, v24\n\t"
    "v_accvgpr_write_b32 a123, v25\n\t"
    "v_exp_f32 v22, v40\n\t"
    "v_exp_f32 v23, v41\n\t"
    "s_nop 0\n\t"
    "v_cvt_pk_bf16_f32 v104, v22, v23\n\t"
    "v_exp_f32 v22, v42\n\t"
    "v_exp_f32 v23, v43\n\t"
    "s_nop 0\n\t"
    "v_cvt_pk_bf16_f32 v105, v22, v23\n\t"
    "v_exp_f32 v22, v56\n\t"
    "v_exp_f32 v23, v57\n\t"
    "s_nop 0\n\t"
    "v_cvt_pk_bf16_f32 v106, v22, v23\n\t"
    "v_exp_f32 v22, v58\n\t"
    "v_exp_f32 v23, v59\n\t"
    "s_nop 0\n\t"
    "v_cvt_pk_bf16_f32 v107, v22, v23\n\t"
    "s_nop 1\n\t"
    "v_mfma_f32_16x16x32_bf16 v[152:155], v[176:179], v[104:107], v[168:171]\n\t"
    "v_max3_f32 v18, v44, v45, v46\n\t"
    "v_max3_f32 v18, v18, v47, v60\n\t"
    "v_max3_f32 v18, v18, v61, v62\n\t"
    "v_max_f32 v18, v18, v63\n\t"
    "v_mov_b32 v19, v18\n\t"
    "s_nop 1\n\t"
    "v_permlane16_swap_b32 v18, v19\n\t"
    "s_nop 1\n\t"
    "v_max_f32 v18, v18, v19\n\t"
    "v_mov_b32 v19, v18\n\t"
    "s_nop 1\n\t"
    "v_permlane32_swap_b32 v18, v19\n\t"
    "s_nop 1\n\t"
    "v_max_f32 v18, v18, v19\n\t"
    "v_add_f32 v18, 0x40e00000, v18\n\t"
    "v_max_f32 v18, 0, v18\n\t"
    "v_mov_b32 v19, 0x43800000\n\t"
    "v_cmp_le_f32 vcc, v19, v172\n\t"
    "v_mov_b32 v19, 0x40c00000\n\t"
    "s_nop 1\n\t"
    "v_cndmask_b32 v19, 0, v19, vcc\n\t"
    "v_max_f32 v18, v18, v19\n\t"
    "v_exp_f32_e64 v20, -v18\n\t"
    "v_sub_f32 v44, v44, v18\n\t"
    "v_sub_f32 v45, v45, v18\n\t"
    "v_sub_f32 v46, v46, v18\n\t"
    "v_sub_f32 v47, v47, v18\n\t"
    "v_sub_f32 v60, v60, v18\n\t"
    "v_sub_f32 v61, v61, v18\n\t"
    "v_sub_f32 v62, v62, v18\n\t"
    "v_sub_f32 v63, v63, v18\n\t"
    "v_sub_f32 v76, v76, v18\n\t"
    "v_sub_f32 v77, v77, v18\n\t"
    "v_sub_f32 v78, v78, v18\n\t"
    "v_sub_f32 v79, v79, v18\n\t"
    "v_sub_f32 v92, v92, v18\n\t"
    "v_sub_f32 v93, v93, v18\n\t"
    "v_sub_f32 v94, v94, v18\n\t"
    "v_sub_f32 v95, v95, v18\n\t"
    "v_sub_f32 v140, v140, v18\n\t"
    "v_sub_f32 v141, v141, v18\n\t"
    "v_sub_f32 v142, v142, v18\n\t"
    "v_sub_f32 v143, v143, v18\n\t"
    "v_mul_f32 v172, v20, v172\n\t"
    "v_mul_f32 v173, v20, v173\n\t"
    "v_mul_f32 v174, v20, v174\n\t"
    "v_mul_f32 v175, v20, v175\n\t"
    "v_accvgpr_read_b32 v22, a12\n\t"
    "v_accvgpr_read_b32 v23, a13\n\t"
    "v_accvgpr_read_b32 v24, a14\n\t"
    "v_accvgpr_read_b32 v25, a15\n\t"
    "v_mul_f32 v22, v20, v22\n\t"
    "v_mul_f32 v23, v20, v23\n\t"
    "v_mul_f32 v24, v20, v24\n\t"
    "v_mul_f32 v25, v20, v25\n\t"
    "v_accvgpr_write_b32 a12, v22\n\t"
    "v_accvgpr_write_b32 a13, v23\n\t"
    "v_accvgpr_write_b32 a14, v24\n\t"
    "v_accvgpr_write_b32 a15, v25\n\t"
    "v_accvgpr_read_b32 v22, a28\n\t"
    "v_accvgpr_read_b32 v23, a29\n\t"
    "v_accvgpr_read_b32 v24, a30\n\t"
    "v_accvgpr_read_b32 v25, a31\n\t"
    "v_mul_f32 v22, v20, v22\n\t"
    "v_mul_f32 v23, v20, v23\n\t"
    "v_mul_f32 v24, v20, v24\n\t"
    "v_mul_f32 v25, v20, v25\n\t"
    "v_accvgpr_write_b32 a28, v22\n\t"
    "v_accvgpr_write_b32 a29, v23\n\t"
    "v_accvgpr_write_b32 a30, v24\n\t"
    "v_accvgpr_write_b32 a31, v25\n\t"
    "v_accvgpr_read_b32 v22, a44\n\t"
    "v_accvgpr_read_b32 v23, a45\n\t"
    "v_accvgpr_read_b32 v24, a46\n\t"
    "v_accvgpr_read_b32 v25, a47\n\t"
    "v_mul_f32 v22, v20, v22\n\t"
    "v_mul_f32 v23, v20, v23\n\t"
    "v_mul_f32 v24, v20, v24\n\t"
    "v_mul_f32 v25, v20, v25\n\t"
    "v_accvgpr_write_b32 a44, v22\n\t"
    "v_accvgpr_write_b32 a45, v23\n\t"
    "v_accvgpr_write_b32 a46, v24\n\t"
    "v_accvgpr_write_b32 a47, v25\n\t"
    "v_accvgpr_read_b32 v22, a60\n\t"
    "v_accvgpr_read_b32 v23, a61\n\t"
    "v_accvgpr_read_b32 v24, a62\n\t"
    "v_accvgpr_read_b32 v25, a63\n\t"
    "v_mul_f32 v22, v20, v22\n\t"
    "v_mul_f32 v23, v20, v23\n\t"
    "v_mul_f32 v24, v20, v24\n\t"
    "v_mul_f32 v25, v20, v25\n\t"
    "v_accvgpr_write_b32 a60, v22\n\t"
    "v_accvgpr_write_b32 a61, v23\n\t"
    "v_accvgpr_write_b32 a62, v24\n\t"
    "v_accvgpr_write_b32 a63, v25\n\t"
    "v_accvgpr_read_b32 v22, a76\n\t"
    "v_accvgpr_read_b32 v23, a77\n\t"
    "v_accvgpr_read_b32 v24, a78\n\t"
    "v_accvgpr_read_b32 v25, a79\n\t"
    "v_mul_f32 v22, v20, v22\n\t"
    "v_mul_f32 v23, v20, v23\n\t"
    "v_mul_f32 v24, v20, v24\n\t"
    "v_mul_f32 v25, v20, v25\n\t"
    "v_accvgpr_write_b32 a76, v22\n\t"
    "v_accvgpr_write_b32 a77, v23\n\t"
    "v_accvgpr_write_b32 a78, v24\n\t"
    "v_accvgpr_write_b32 a79, v25\n\t"
    "v_accvgpr_read_b32 v22, a92\n\t"
    "v_accvgpr_read_b32 v23, a93\n\t"
    "v_accvgpr_read_b32 v24, a94\n\t"
    "v_accvgpr_read_b32 v25, a95\n\t"
    "v_mul_f32 v22, v20, v22\n\t"
    "v_mul_f32 v23, v20, v23\n\t"
    "v_mul_f32 v24, v20, v24\n\t"
    "v_mul_f32 v25, v20, v25\n\t"
    "v_accvgpr_write_b32 a92, v22\n\t"
    "v_accvgpr_write_b32 a93, v23\n\t"
    "v_accvgpr_write_b32 a94, v24\n\t"
    "v_accvgpr_write_b32 a95, v25\n\t"
    "v_accvgpr_read_b32 v22, a108\n\t"
    "v_accvgpr_read_b32 v23, a109\n\t"
    "v_accvgpr_read_b32 v24, a110\n\t"
    "v_accvgpr_read_b32 v25, a111\n\t"
    "v_mul_f32 v22, v20, v22\n\t"
    "v_mul_f32 v23, v20, v23\n\t"
    "v_mul_f32 v24, v20, v24\n\t"
    "v_mul_f32 v25, v20, v25\n\t"
    "v_accvgpr_write_b32 a108, v22\n\t"
    "v_accvgpr_write_b32 a109, v23\n\t"
    "v_accvgpr_write_b32 a110, v24\n\t"
    "v_accvgpr_write_b32 a111, v25\n\t"
    "v_accvgpr_read_b32 v22, a124\n\t"
    "v_accvgpr_read_b32 v23, a125\n\t"
    "v_accvgpr_read_b32 v24, a126\n\t"
    "v_accvgpr_read_b32 v25, a127\n\t"
    "v_mul_f32 v22, v20, v22\n\t"
    "v_mul_f32 v23, v20, v23\n\t"
    "v_mul_f32 v24, v20, v24\n\t"
    "v_mul_f32 v25, v20, v25\n\t"
    "v_accvgpr_write_b32 a124, v22\n\t"
    "v_accvgpr_write_b32 a125, v23\n\t"
    "v_accvgpr_write_b32 a126, v24\n\t"
    "v_accvgpr_write_b32 a127, v25\n\t"
    "v_exp_f32 v22, v44\n\t"
    "v_exp_f32 v23, v45\n\t"
    "s_nop 0\n\t"
    "v_cvt_pk_bf16_f32 v108, v22, v23\n\t"
    "v_exp_f32 v22, v46\n\t"
    "v_exp_f32 v23, v47\n\t"
    "s_nop 0\n\t"
    "v_cvt_pk_bf16_f32 v109, v22, v23\n\t"
    "v_exp_f32 v22, v60\n\t"
    "v_exp_f32 v23, v61\n\t"
    "s_nop 0\n\t"
    "v_cvt_pk_bf16_f32 v110, v22, v23\n\t"
    "v_exp_f32 v22, v62\n\t"
    "v_exp_f32 v23, v63\n\t"
    "s_nop 0\n\t"
    "v_cvt_pk_bf16_f32 v111, v22, v23\n\t"
    "s_nop 1\n\t"
    "v_mfma_f32_16x16x32_bf16 v[156:159], v[176:179], v[108:111], v[172:175]\n\t"
    "s_nop 7\n\t"
    "s_setpc_b64 s[34:35]\n\t"
    "L_rare1sub_%=:\n\t"
    "s_nop 15\n\t"
    "v_max3_f32 v18, v64, v65, v66\n\t"
    "v_max3_f32 v18, v18, v67, v80\n\t"
    "v_max3_f32 v18, v18, v81, v82\n\t"
    "v_max_f32 v18, v18, v83\n\t"
    "v_mov_b32 v19, v18\n\t"
    "s_nop 1\n\t"
    "v_permlane16_swap_b32 v18, v19\n\t"
    "s_nop 1\n\t"
    "v_max_f32 v18, v18, v19\n\t"
    "v_mov_b32 v19, v18\n\t"
    "s_nop 1\n\t"
    "v_permlane32_swap_b32 v18, v19\n\t"
    "s_nop 1\n\t"
    "v_max_f32 v18, v18, v19\n\t"
    "v_add_f32 v18, 0x40e00000, v18\n\t"
    "v_max_f32 v18, 0, v18\n\t"
    "v_mov_b32 v19, 0x43800000\n\t"
    "v_cmp_le_f32 vcc, v19, v144\n\t"
    "v_mov_b32 v19, 0x40c00000\n\t"
    "s_nop 1\n\t"
    "v_cndmask_b32 v19, 0, v19, vcc\n\t"
    "v_max_f32 v18, v18, v19\n\t"
    "v_exp_f32_e64 v20, -v18\n\t"
    "v_sub_f32 v64, v64, v18\n\t"
    "v_sub_f32 v65, v65, v18\n\t"
    "v_sub_f32 v66, v66, v18\n\t"
    "v_sub_f32 v67, v67, v18\n\t"
    "v_sub_f32 v80, v80, v18\n\t"
    "v_sub_f32 v81, v81, v18\n\t"
    "v_sub_f32 v82, v82, v18\n\t"
    "v_sub_f32 v83, v83, v18\n\t"
    "v_sub_f32 v32, v32, v18\n\t"
    "v_sub_f32 v33, v33, v18\n\t"
    "v_sub_f32 v34, v34, v18\n\t"
    "v_sub_f32 v35, v35, v18\n\t"
    "v_sub_f32 v48, v48, v18\n\t"
    "v_sub_f32 v49, v49, v18\n\t"
    "v_sub_f32 v50, v50, v18\n\t"
    "v_sub_f32 v51, v51, v18\n\t"
    "v_sub_f32 v128, v128, v18\n\t"
    "v_sub_f32 v129, v129, v18\n\t"
    "v_sub_f32 v130, v130, v18\n\t"
    "v_sub_f32 v131, v131, v18\n\t"
    "v_mul_f32 v144, v20, v144\n\t"
    "v_mul_f32 v145, v20, v145\n\t"
    "v_mul_f32 v146, v20, v146\n\t"
    "v_mul_f32 v147, v20, v147\n\t"
    "v_accvgpr_read_b32 v22, a0\n\t"
    "v_accvgpr_read_b32 v23, a1\n\t"
    "v_accvgpr_read_b32 v24, a2\n\t"
    "v_accvgpr_read_b32 v25, a3\n\t"
    "v_mul_f32 v22, v20, v22\n\t"
    "v_mul_f32 v23, v20, v23\n\t"
    "v_mul_f32 v24, v20, v24\n\t"
    "v_mul_f32 v25, v20, v25\n\t"
    "v_accvgpr_write_b32 a0, v22\n\t"
    "v_accvgpr_write_b32 a1, v23\n\t"
    "v_accvgpr_write_b32 a2, v24\n\t"
    "v_accvgpr_write_b32 a3, v25\n\t"
    "v_accvgpr_read_b32 v22, a16\n\t"
    "v_accvgpr_read_b32 v23, a17\n\t"
    "v_accvgpr_read_b32 v24, a18\n\t"
    "v_accvgpr_read_b32 v25, a19\n\t"
    "v_mul_f32 v22, v20, v22\n\t"
    "v_mul_f32 v23, v20, v23\n\t"
    "v_mul_f32 v24, v20, v24\n\t"
    "v_mul_f32 v25, v20, v25\n\t"
    "v_accvgpr_write_b32 a16, v22\n\t"
    "v_accvgpr_write_b32 a17, v23\n\t"
    "v_accvgpr_write_b32 a18, v24\n\t"
    "v_accvgpr_write_b32 a19, v25\n\t"
    "v_accvgpr_read_b32 v22, a32\n\t"
    "v_accvgpr_read_b32 v23, a33\n\t"
    "v_accvgpr_read_b32 v24, a34\n\t"
    "v_accvgpr_read_b32 v25, a35\n\t"
    "v_mul_f32 v22, v20, v22\n\t"
    "v_mul_f32 v23, v20, v23\n\t"
    "v_mul_f32 v24, v20, v24\n\t"
    "v_mul_f32 v25, v20, v25\n\t"
    "v_accvgpr_write_b32 a32, v22\n\t"
    "v_accvgpr_write_b32 a33, v23\n\t"
    "v_accvgpr_write_b32 a34, v24\n\t"
    "v_accvgpr_write_b32 a35, v25\n\t"
    "v_accvgpr_read_b32 v22, a48\n\t"
    "v_accvgpr_read_b32 v23, a49\n\t"
    "v_accvgpr_read_b32 v24, a50\n\t"
    "v_accvgpr_read_b32 v25, a51\n\t"
    "v_mul_f32 v22, v20, v22\n\t"
    "v_mul_f32 v23, v20, v23\n\t"
    "v_mul_f32 v24, v20, v24\n\t"
    "v_mul_f32 v25, v20, v25\n\t"
    "v_accvgpr_write_b32 a48, v22\n\t"
    "v_accvgpr_write_b32 a49, v23\n\t"
    "v_accvgpr_write_b32 a50, v24\n\t"
    "v_accvgpr_write_b32 a51, v25\n\t"
    "v_accvgpr_read_b32 v22, a64\n\t"
    "v_accvgpr_read_b32 v23, a65\n\t"
    "v_accvgpr_read_b32 v24, a66\n\t"
    "v_accvgpr_read_b32 v25, a67\n\t"
    "v_mul_f32 v22, v20, v22\n\t"
    "v_mul_f32 v23, v20, v23\n\t"
    "v_mul_f32 v24, v20, v24\n\t"
    "v_mul_f32 v25, v20, v25\n\t"
    "v_accvgpr_write_b32 a64, v22\n\t"
    "v_accvgpr_write_b32 a65, v23\n\t"
    "v_accvgpr_write_b32 a66, v24\n\t"
    "v_accvgpr_write_b32 a67, v25\n\t"
    "v_accvgpr_read_b32 v22, a80\n\t"
    "v_accvgpr_read_b32 v23, a81\n\t"
    "v_accvgpr_read_b32 v24, a82\n\t"
    "v_accvgpr_read_b32 v25, a83\n\t"
    "v_mul_f32 v22, v20, v22\n\t"
    "v_mul_f32 v23, v20, v23\n\t"
    "v_mul_f32 v24, v20, v24\n\t"
    "v_mul_f32 v25, v20, v25\n\t"
    "v_accvgpr_write_b32 a80, v22\n\t"
    "v_accvgpr_write_b32 a81, v23\n\t"
    "v_accvgpr_write_b32 a82, v24\n\t"
    "v_accvgpr_write_b32 a83, v25\n\t"
    "v_accvgpr_read_b32 v22, a96\n\t"
    "v_accvgpr_read_b32 v23, a97\n\t"
    "v_accvgpr_read_b32 v24, a98\n\t"
    "v_accvgpr_read_b32 v25, a99\n\t"
    "v_mul_f32 v22, v20, v22\n\t"
    "v_mul_f32 v23, v20, v23\n\t"
    "v_mul_f32 v24, v20, v24\n\t"
    "v_mul_f32 v25, v20, v25\n\t"
    "v_accvgpr_write_b32 a96, v22\n\t"
    "v_accvgpr_write_b32 a97, v23\n\t"
    "v_accvgpr_write_b32 a98, v24\n\t"
    "v_accvgpr_write_b32 a99, v25\n\t"
    "v_accvgpr_read_b32 v22, a112\n\t"
    "v_accvgpr_read_b32 v23, a113\n\t"
    "v_accvgpr_read_b32 v24, a114\n\t"
    "v_accvgpr_read_b32 v25, a115\n\t"
    "v_mul_f32 v22, v20, v22\n\t"
    "v_mul_f32 v23, v20, v23\n\t"
    "v_mul_f32 v24, v20, v24\n\t"
    "v_mul_f32 v25, v20, v25\n\t"
    "v_accvgpr_write_b32 a112, v22\n\t"
    "v_accvgpr_write_b32 a113, v23\n\t"
    "v_accvgpr_write_b32 a114, v24\n\t"
    "v_accvgpr_write_b32 a115, v25\n\t"
    "v_exp_f32 v22, v64\n\t"
    "v_exp_f32 v23, v65\n\t"
    "s_nop 0\n\t"
    "v_cvt_pk_bf16_f32 v112, v22, v23\n\t"
    "v_exp_f32 v22, v66\n\t"
    "v_exp_f32 v23, v67\n\t"
    "s_nop 0\n\t"
    "v_cvt_pk_bf16_f32 v113, v22, v23\n\t"
    "v_exp_f32 v22, v80\n\t"
    "v_exp_f32 v23, v81\n\t"
    "s_nop 0\n\t"
    "v_cvt_pk_bf16_f32 v114, v22, v23\n\t"
    "v_exp_f32 v22, v82\n\t"
    "v_exp_f32 v23, v83\n\t"
    "s_nop 0\n\t"
    "v_cvt_pk_bf16_f32 v115, v22, v23\n\t"
    "s_nop 1\n\t"
    "v_mfma_f32_16x16x32_bf16 v[160:163], v[176:179], v[112:115], v[144:147]\n\t"
    "v_max3_f32 v18, v68, v69, v70\n\t"
    "v_max3_f32 v18, v18, v71, v84\n\t"
    "v_max3_f32 v18, v18, v85, v86\n\t"
    "v_max_f32 v18, v18, v87\n\t"
    "v_mov_b32 v19, v18\n\t"
    "s_nop 1\n\t"
    "v_permlane16_swap_b32 v18, v19\n\t"
    "s_nop 1\n\t"
    "v_max_f32 v18, v18, v19\n\t"
    "v_mov_b32 v19, v18\n\t"
    "s_nop 1\n\t"
    "v_permlane32_swap_b32 v18, v19\n\t"
    "s_nop 1\n\t"
    "v_max_f32 v18, v18, v19\n\t"
    "v_add_f32 v18, 0x40e00000, v18\n\t"
    "v_max_f32 v18, 0, v18\n\t"
    "v_mov_b32 v19, 0x43800000\n\t"
    "v_cmp_le_f32 vcc, v19, v148\n\t"
    "v_mov_b32 v19, 0x40c00000\n\t"
    "s_nop 1\n\t"
    "v_cndmask_b32 v19, 0, v19, vcc\n\t"
    "v_max_f32 v18, v18, v19\n\t"
    "v_exp_f32_e64 v20, -v18\n\t"
    "v_sub_f32 v68, v68, v18\n\t"
    "v_sub_f32 v69, v69, v18\n\t"
    "v_sub_f32 v70, v70, v18\n\t"
    "v_sub_f32 v71, v71, v18\n\t"
    "v_sub_f32 v84, v84, v18\n\t"
    "v_sub_f32 v85, v85, v18\n\t"
    "v_sub_f32 v86, v86, v18\n\t"
    "v_sub_f32 v87, v87, v18\n\t"
    "v_sub_f32 v36, v36, v18\n\t"
    "v_sub_f32 v37, v37, v18\n\t"
    "v_sub_f32 v38, v38, v18\n\t"
    "v_sub_f32 v39, v39, v18\n\t"
    "v_sub_f32 v52, v52, v18\n\t"
    "v_sub_f32 v53, v53, v18\n\t"
    "v_sub_f32 v54, v54, v18\n\t"
    "v_sub_f32 v55, v55, v18\n\t"
    "v_sub_f32 v132, v132, v18\n\t"
    "v_sub_f32 v133, v133, v18\n\t"
    "v_sub_f32 v134, v134, v18\n\t"
    "v_sub_f32 v135, v135, v18\n\t"
    "v_mul_f32 v148, v20, v148\n\t"
    "v_mul_f32 v149, v20, v149\n\t"
    "v_mul_f32 v150, v20, v150\n\t"
    "v_mul_f32 v151, v20, v151\n\t"
    "v_accvgpr_read_b32 v22, a4\n\t"
    "v_accvgpr_read_b32 v23, a5\n\t"
    "v_accvgpr_read_b32 v24, a6\n\t"
    "v_accvgpr_read_b32 v25, a7\n\t"
    "v_mul_f32 v22, v20, v22\n\t"
    "v_mul_f32 v23, v20, v23\n\t"
    "v_mul_f32 v24, v20, v24\n\t"
    "v_mul_f32 v25, v20, v25\n\t"
    "v_accvgpr_write_b32 a4, v22\n\t"
    "v_accvgpr_write_b32 a5, v23\n\t"
    "v_accvgpr_write_b32 a6, v24\n\t"
    "v_accvgpr_write_b32 a7, v25\n\t"
    "v_accvgpr_read_b32 v22, a20\n\t"
    "v_accvgpr_read_b32 v23, a21\n\t"
    "v_accvgpr_read_b32 v24, a22\n\t"
    "v_accvgpr_read_b32 v25, a23\n\t"
    "v_mul_f32 v22, v20, v22\n\t"
    "v_mul_f32 v23, v20, v23\n\t"
    "v_mul_f32 v24, v20, v24\n\t"
    "v_mul_f32 v25, v20, v25\n\t"
    "v_accvgpr_write_b32 a20, v22\n\t"
    "v_accvgpr_write_b32 a21, v23\n\t"
    "v_accvgpr_write_b32 a22, v24\n\t"
    "v_accvgpr_write_b32 a23, v25\n\t"
    "v_accvgpr_read_b32 v22, a36\n\t"
    "v_accvgpr_read_b32 v23, a37\n\t"
    "v_accvgpr_read_b32 v24, a38\n\t"
    "v_accvgpr_read_b32 v25, a39\n\t"
    "v_mul_f32 v22, v20, v22\n\t"
    "v_mul_f32 v23, v20, v23\n\t"
    "v_mul_f32 v24, v20, v24\n\t"
    "v_mul_f32 v25, v20, v25\n\t"
    "v_accvgpr_write_b32 a36, v22\n\t"
    "v_accvgpr_write_b32 a37, v23\n\t"
    "v_accvgpr_write_b32 a38, v24\n\t"
    "v_accvgpr_write_b32 a39, v25\n\t"
    "v_accvgpr_read_b32 v22, a52\n\t"
    "v_accvgpr_read_b32 v23, a53\n\t"
    "v_accvgpr_read_b32 v24, a54\n\t"
    "v_accvgpr_read_b32 v25, a55\n\t"
    "v_mul_f32 v22, v20, v22\n\t"
    "v_mul_f32 v23, v20, v23\n\t"
    "v_mul_f32 v24, v20, v24\n\t"
    "v_mul_f32 v25, v20, v25\n\t"
    "v_accvgpr_write_b32 a52, v22\n\t"
    "v_accvgpr_write_b32 a53, v23\n\t"
    "v_accvgpr_write_b32 a54, v24\n\t"
    "v_accvgpr_write_b32 a55, v25\n\t"
    "v_accvgpr_read_b32 v22, a68\n\t"
    "v_accvgpr_read_b32 v23, a69\n\t"
    "v_accvgpr_read_b32 v24, a70\n\t"
    "v_accvgpr_read_b32 v25, a71\n\t"
    "v_mul_f32 v22, v20, v22\n\t"
    "v_mul_f32 v23, v20, v23\n\t"
    "v_mul_f32 v24, v20, v24\n\t"
    "v_mul_f32 v25, v20, v25\n\t"
    "v_accvgpr_write_b32 a68, v22\n\t"
    "v_accvgpr_write_b32 a69, v23\n\t"
    "v_accvgpr_write_b32 a70, v24\n\t"
    "v_accvgpr_write_b32 a71, v25\n\t"
    "v_accvgpr_read_b32 v22, a84\n\t"
    "v_accvgpr_read_b32 v23, a85\n\t"
    "v_accvgpr_read_b32 v24, a86\n\t"
    "v_accvgpr_read_b32 v25, a87\n\t"
    "v_mul_f32 v22, v20, v22\n\t"
    "v_mul_f32 v23, v20, v23\n\t"
    "v_mul_f32 v24, v20, v24\n\t"
    "v_mul_f32 v25, v20, v25\n\t"
    "v_accvgpr_write_b32 a84, v22\n\t"
    "v_accvgpr_write_b32 a85, v23\n\t"
    "v_accvgpr_write_b32 a86, v24\n\t"
    "v_accvgpr_write_b32 a87, v25\n\t"
    "v_accvgpr_read_b32 v22, a100\n\t"
    "v_accvgpr_read_b32 v23, a101\n\t"
    "v_accvgpr_read_b32 v24, a102\n\t"
    "v_accvgpr_read_b32 v25, a103\n\t"
    "v_mul_f32 v22, v20, v22\n\t"
    "v_mul_f32 v23, v20, v23\n\t"
    "v_mul_f32 v24, v20, v24\n\t"
    "v_mul_f32 v25, v20, v25\n\t"
    "v_accvgpr_write_b32 a100, v22\n\t"
    "v_accvgpr_write_b32 a101, v23\n\t"
    "v_accvgpr_write_b32 a102, v24\n\t"
    "v_accvgpr_write_b32 a103, v25\n\t"
    "v_accvgpr_read_b32 v22, a116\n\t"
    "v_accvgpr_read_b32 v23, a117\n\t"
    "v_accvgpr_read_b32 v24, a118\n\t"
    "v_accvgpr_read_b32 v25, a119\n\t"
    "v_mul_f32 v22, v20, v22\n\t"
    "v_mul_f32 v23, v20, v23\n\t"
    "v_mul_f32 v24, v20, v24\n\t"
    "v_mul_f32 v25, v20, v25\n\t"
    "v_accvgpr_write_b32 a116, v22\n\t"
    "v_accvgpr_write_b32 a117, v23\n\t"
    "v_accvgpr_write_b32 a118, v24\n\t"
    "v_accvgpr_write_b32 a119, v25\n\t"
    "v_exp_f32 v22, v68\n\t"
    "v_exp_f32 v23, v69\n\t"
    "s_nop 0\n\t"
    "v_cvt_pk_bf16_f32 v116, v22, v23\n\t"
    "v_exp_f32 v22, v70\n\t"
    "v_exp_f32 v23, v71\n\t"
    "s_nop 0\n\t"
    "v_cvt_pk_bf16_f32 v117, v22, v23\n\t"
    "v_exp_f32 v22, v84\n\t"
    "v_exp_f32 v23, v85\n\t"
    "s_nop 0\n\t"
    "v_cvt_pk_bf16_f32 v118, v22, v23\n\t"
    "v_exp_f32 v22, v86\n\t"
    "v_exp_f32 v23, v87\n\t"
    "s_nop 0\n\t"
    "v_cvt_pk_bf16_f32 v119, v22, v23\n\t"
    "s_nop 1\n\t"
    "v_mfma_f32_16x16x32_bf16 v[164:167], v[176:179], v[116:119], v[148:151]\n\t"
    "v_max3_f32 v18, v72, v73, v74\n\t"
    "v_max3_f32 v18, v18, v75, v88\n\t"
    "v_max3_f32 v18, v18, v89, v90\n\t"
    "v_max_f32 v18, v18, v91\n\t"
    "v_mov_b32 v19, v18\n\t"
    "s_nop 1\n\t"
    "v_permlane16_swap_b32 v18, v19\n\t"
    "s_nop 1\n\t"
    "v_max_f32 v18, v18, v19\n\t"
    "v_mov_b32 v19, v18\n\t"
    "s_nop 1\n\t"
    "v_permlane32_swap_b32 v18, v19\n\t"
    "s_nop 1\n\t"
    "v_max_f32 v18, v18, v19\n\t"
    "v_add_f32 v18, 0x40e00000, v18\n\t"
    "v_max_f32 v18, 0, v18\n\t"
    "v_mov_b32 v19, 0x43800000\n\t"
    "v_cmp_le_f32 vcc, v19, v152\n\t"
    "v_mov_b32 v19, 0x40c00000\n\t"
    "s_nop 1\n\t"
    "v_cndmask_b32 v19, 0, v19, vcc\n\t"
    "v_max_f32 v18, v18, v19\n\t"
    "v_exp_f32_e64 v20, -v18\n\t"
    "v_sub_f32 v72, v72, v18\n\t"
    "v_sub_f32 v73, v73, v18\n\t"
    "v_sub_f32 v74, v74, v18\n\t"
    "v_sub_f32 v75, v75, v18\n\t"
    "v_sub_f32 v88, v88, v18\n\t"
    "v_sub_f32 v89, v89, v18\n\t"
    "v_sub_f32 v90, v90, v18\n\t"
    "v_sub_f32 v91, v91, v18\n\t"
    "v_sub_f32 v40, v40, v18\n\t"
    "v_sub_f32 v41, v41, v18\n\t"
    "v_sub_f32 v42, v42, v18\n\t"
    "v_sub_f32 v43, v43, v18\n\t"
    "v_sub_f32 v56, v56, v18\n\t"
    "v_sub_f32 v57, v57, v18\n\t"
    "v_sub_f32 v58, v58, v18\n\t"
    "v_sub_f32 v59, v59, v18\n\t"
    "v_sub_f32 v136, v136, v18\n\t"
    "v_sub_f32 v137, v137, v18\n\t"
    "v_sub_f32 v138, v138, v18\n\t"
    "v_sub_f32 v139, v139, v18\n\t"
    "v_mul_f32 v152, v20, v152\n\t"
    "v_mul_f32 v153, v20, v153\n\t"
    "v_mul_f32 v154, v20, v154\n\t"
    "v_mul_f32 v155, v20, v155\n\t"
    "v_accvgpr_read_b32 v22, a8\n\t"
    "v_accvgpr_read_b32 v23, a9\n\t"
    "v_accvgpr_read_b32 v24, a10\n\t"
    "v_accvgpr_read_b32 v25, a11\n\t"
    "v_mul_f32 v22, v20, v22\n\t"
    "v_mul_f32 v23, v20, v23\n\t"
    "v_mul_f32 v24, v20, v24\n\t"
    "v_mul_f32 v25, v20, v25\n\t"
    "v_accvgpr_write_b32 a8, v22\n\t"
    "v_accvgpr_write_b32 a9, v23\n\t"
    "v_accvgpr_write_b32 a10, v24\n\t"
    "v_accvgpr_write_b32 a11, v25\n\t"
    "v_accvgpr_read_b32 v22, a24\n\t"
    "v_accvgpr_read_b32 v23, a25\n\t"
    "v_accvgpr_read_b32 v24, a26\n\t"
    "v_accvgpr_read_b32 v25, a27\n\t"
    "v_mul_f32 v22, v20, v22\n\t"
    "v_mul_f32 v23, v20, v23\n\t"
    "v_mul_f32 v24, v20, v24\n\t"
    "v_mul_f32 v25, v20, v25\n\t"
    "v_accvgpr_write_b32 a24, v22\n\t"
    "v_accvgpr_write_b32 a25, v23\n\t"
    "v_accvgpr_write_b32 a26, v24\n\t"
    "v_accvgpr_write_b32 a27, v25\n\t"
    "v_accvgpr_read_b32 v22, a40\n\t"
    "v_accvgpr_read_b32 v23, a41\n\t"
    "v_accvgpr_read_b32 v24, a42\n\t"
    "v_accvgpr_read_b32 v25, a43\n\t"
    "v_mul_f32 v22, v20, v22\n\t"
    "v_mul_f32 v23, v20, v23\n\t"
    "v_mul_f32 v24, v20, v24\n\t"
    "v_mul_f32 v25, v20, v25\n\t"
    "v_accvgpr_write_b32 a40, v22\n\t"
    "v_accvgpr_write_b32 a41, v23\n\t"
    "v_accvgpr_write_b32 a42, v24\n\t"
    "v_accvgpr_write_b32 a43, v25\n\t"
    "v_accvgpr_read_b32 v22, a56\n\t"
    "v_accvgpr_read_b32 v23, a57\n\t"
    "v_accvgpr_read_b32 v24, a58\n\t"
    "v_accvgpr_read_b32 v25, a59\n\t"
    "v_mul_f32 v22, v20, v22\n\t"
    "v_mul_f32 v23, v20, v23\n\t"
    "v_mul_f32 v24, v20, v24\n\t"
    "v_mul_f32 v25, v20, v25\n\t"
    "v_accvgpr_write_b32 a56, v22\n\t"
    "v_accvgpr_write_b32 a57, v23\n\t"
    "v_accvgpr_write_b32 a58, v24\n\t"
    "v_accvgpr_write_b32 a59, v25\n\t"
    "v_accvgpr_read_b32 v22, a72\n\t"
    "v_accvgpr_read_b32 v23, a73\n\t"
    "v_accvgpr_read_b32 v24, a74\n\t"
    "v_accvgpr_read_b32 v25, a75\n\t"
    "v_mul_f32 v22, v20, v22\n\t"
    "v_mul_f32 v23, v20, v23\n\t"
    "v_mul_f32 v24, v20, v24\n\t"
    "v_mul_f32 v25, v20, v25\n\t"
    "v_accvgpr_write_b32 a72, v22\n\t"
    "v_accvgpr_write_b32 a73, v23\n\t"
    "v_accvgpr_write_b32 a74, v24\n\t"
    "v_accvgpr_write_b32 a75, v25\n\t"
    "v_accvgpr_read_b32 v22, a88\n\t"
    "v_accvgpr_read_b32 v23, a89\n\t"
    "v_accvgpr_read_b32 v24, a90\n\t"
    "v_accvgpr_read_b32 v25, a91\n\t"
    "v_mul_f32 v22, v20, v22\n\t"
    "v_mul_f32 v23, v20, v23\n\t"
    "v_mul_f32 v24, v20, v24\n\t"
    "v_mul_f32 v25, v20, v25\n\t"
    "v_accvgpr_write_b32 a88, v22\n\t"
    "v_accvgpr_write_b32 a89, v23\n\t"
    "v_accvgpr_write_b32 a90, v24\n\t"
    "v_accvgpr_write_b32 a91, v25\n\t"
    "v_accvgpr_read_b32 v22, a104\n\t"
    "v_accvgpr_read_b32 v23, a105\n\t"
    "v_accvgpr_read_b32 v24, a106\n\t"
    "v_accvgpr_read_b32 v25, a107\n\t"
    "v_mul_f32 v22, v20, v22\n\t"
    "v_mul_f32 v23, v20, v23\n\t"
    "v_mul_f32 v24, v20, v24\n\t"
    "v_mul_f32 v25, v20, v25\n\t"
    "v_accvgpr_write_b32 a104, v22\n\t"
    "v_accvgpr_write_b32 a105, v23\n\t"
    "v_accvgpr_write_b32 a106, v24\n\t"
    "v_accvgpr_write_b32 a107, v25\n\t"
    "v_accvgpr_read_b32 v22, a120\n\t"
    "v_accvgpr_read_b32 v23, a121\n\t"
    "v_accvgpr_read_b32 v24, a122\n\t"
    "v_accvgpr_read_b32 v25, a123\n\t"
    "v_mul_f32 v22, v20, v22\n\t"
    "v_mul_f32 v23, v20, v23\n\t"
    "v_mul_f32 v24, v20, v24\n\t"
    "v_mul_f32 v25, v20, v25\n\t"
    "v_accvgpr_write_b32 a120, v22\n\t"
    "v_accvgpr_write_b32 a121, v23\n\t"
    "v_accvgpr_write_b32 a122, v24\n\t"
    "v_accvgpr_write_b32 a123, v25\n\t"
    "v_exp_f32 v22, v72\n\t"
    "v_exp_f32 v23, v73\n\t"
    "s_nop 0\n\t"
    "v_cvt_pk_bf16_f32 v120, v22, v23\n\t"
    "v_exp_f32 v22, v74\n\t"
    "v_exp_f32 v23, v75\n\t"
    "s_nop 0\n\t"
    "v_cvt_pk_bf16_f32 v121, v22, v23\n\t"
    "v_exp_f32 v22, v88\n\t"
    "v_exp_f32 v23, v89\n\t"
    "s_nop 0\n\t"
    "v_cvt_pk_bf16_f32 v122, v22, v23\n\t"
    "v_exp_f32 v22, v90\n\t"
    "v_exp_f32 v23, v91\n\t"
    "s_nop 0\n\t"
    "v_cvt_pk_bf16_f32 v123, v22, v23\n\t"
    "s_nop 1\n\t"
    "v_mfma_f32_16x16x32_bf16 v[168:171], v[176:179], v[120:123], v[152:155]\n\t"
    "v_max3_f32 v18, v76, v77, v78\n\t"
    "v_max3_f32 v18, v18, v79, v92\n\t"
    "v_max3_f32 v18, v18, v93, v94\n\t"
    "v_max_f32 v18, v18, v95\n\t"
    "v_mov_b32 v19, v18\n\t"
    "s_nop 1\n\t"
    "v_permlane16_swap_b32 v18, v19\n\t"
    "s_nop 1\n\t"
    "v_max_f32 v18, v18, v19\n\t"
    "v_mov_b32 v19, v18\n\t"
    "s_nop 1\n\t"
    "v_permlane32_swap_b32 v18, v19\n\t"
    "s_nop 1\n\t"
    "v_max_f32 v18, v18, v19\n\t"
    "v_add_f32 v18, 0x40e00000, v18\n\t"
    "v_max_f32 v18, 0, v18\n\t"
    "v_mov_b32 v19, 0x43800000\n\t"
    "v_cmp_le_f32 vcc, v19, v156\n\t"
    "v_mov_b32 v19, 0x40c00000\n\t"
    "s_nop 1\n\t"
    "v_cndmask_b32 v19, 0, v19, vcc\n\t"
    "v_max_f32 v18, v18, v19\n\t"
    "v_exp_f32_e64 v20, -v18\n\t"
    "v_sub_f32 v76, v76, v18\n\t"
    "v_sub_f32 v77, v77, v18\n\t"
    "v_sub_f32 v78, v78, v18\n\t"
    "v_sub_f32 v79, v79, v18\n\t"
    "v_sub_f32 v92, v92, v18\n\t"
    "v_sub_f32 v93, v93, v18\n\t"
    "v_sub_f32 v94, v94, v18\n\t"
    "v_sub_f32 v95, v95, v18\n\t"
    "v_sub_f32 v44, v44, v18\n\t"
    "v_sub_f32 v45, v45, v18\n\t"
    "v_sub_f32 v46, v46, v18\n\t"
    "v_sub_f32 v47, v47, v18\n\t"
    "v_sub_f32 v60, v60, v18\n\t"
    "v_sub_f32 v61, v61, v18\n\t"
    "v_sub_f32 v62, v62, v18\n\t"
    "v_sub_f32 v63, v63, v18\n\t"
    "v_sub_f32 v140, v140, v18\n\t"
    "v_sub_f32 v141, v141, v18\n\t"
    "v_sub_f32 v142, v142, v18\n\t"
    "v_sub_f32 v143, v143, v18\n\t"
    "v_mul_f32 v156, v20, v156\n\t"
    "v_mul_f32 v157, v20, v157\n\t"
    "v_mul_f32 v158, v20, v158\n\t"
    "v_mul_f32 v159, v20, v159\n\t"
    "v_accvgpr_read_b32 v22, a12\n\t"
    "v_accvgpr_read_b32 v23, a13\n\t"
    "v_accvgpr_read_b32 v24, a14\n\t"
    "v_accvgpr_read_b32 v25, a15\n\t"
    "v_mul_f32 v22, v20, v22\n\t"
    "v_mul_f32 v23, v20, v23\n\t"
    "v_mul_f32 v24, v20, v24\n\t"
    "v_mul_f32 v25, v20, v25\n\t"
    "v_accvgpr_write_b32 a12, v22\n\t"
    "v_accvgpr_write_b32 a13, v23\n\t"
    "v_accvgpr_write_b32 a14, v24\n\t"
    "v_accvgpr_write_b32 a15, v25\n\t"
    "v_accvgpr_read_b32 v22, a28\n\t"
    "v_accvgpr_read_b32 v23, a29\n\t"
    "v_accvgpr_read_b32 v24, a30\n\t"
    "v_accvgpr_read_b32 v25, a31\n\t"
    "v_mul_f32 v22, v20, v22\n\t"
    "v_mul_f32 v23, v20, v23\n\t"
    "v_mul_f32 v24, v20, v24\n\t"
    "v_mul_f32 v25, v20, v25\n\t"
    "v_accvgpr_write_b32 a28, v22\n\t"
    "v_accvgpr_write_b32 a29, v23\n\t"
    "v_accvgpr_write_b32 a30, v24\n\t"
    "v_accvgpr_write_b32 a31, v25\n\t"
    "v_accvgpr_read_b32 v22, a44\n\t"
    "v_accvgpr_read_b32 v23, a45\n\t"
    "v_accvgpr_read_b32 v24, a46\n\t"
    "v_accvgpr_read_b32 v25, a47\n\t"
    "v_mul_f32 v22, v20, v22\n\t"
    "v_mul_f32 v23, v20, v23\n\t"
    "v_mul_f32 v24, v20, v24\n\t"
    "v_mul_f32 v25, v20, v25\n\t"
    "v_accvgpr_write_b32 a44, v22\n\t"
    "v_accvgpr_write_b32 a45, v23\n\t"
    "v_accvgpr_write_b32 a46, v24\n\t"
    "v_accvgpr_write_b32 a47, v25\n\t"
    "v_accvgpr_read_b32 v22, a60\n\t"
    "v_accvgpr_read_b32 v23, a61\n\t"
    "v_accvgpr_read_b32 v24, a62\n\t"
    "v_accvgpr_read_b32 v25, a63\n\t"
    "v_mul_f32 v22, v20, v22\n\t"
    "v_mul_f32 v23, v20, v23\n\t"
    "v_mul_f32 v24, v20, v24\n\t"
    "v_mul_f32 v25, v20, v25\n\t"
    "v_accvgpr_write_b32 a60, v22\n\t"
    "v_accvgpr_write_b32 a61, v23\n\t"
    "v_accvgpr_write_b32 a62, v24\n\t"
    "v_accvgpr_write_b32 a63, v25\n\t"
    "v_accvgpr_read_b32 v22, a76\n\t"
    "v_accvgpr_read_b32 v23, a77\n\t"
    "v_accvgpr_read_b32 v24, a78\n\t"
    "v_accvgpr_read_b32 v25, a79\n\t"
    "v_mul_f32 v22, v20, v22\n\t"
    "v_mul_f32 v23, v20, v23\n\t"
    "v_mul_f32 v24, v20, v24\n\t"
    "v_mul_f32 v25, v20, v25\n\t"
    "v_accvgpr_write_b32 a76, v22\n\t"
    "v_accvgpr_write_b32 a77, v23\n\t"
    "v_accvgpr_write_b32 a78, v24\n\t"
    "v_accvgpr_write_b32 a79, v25\n\t"
    "v_accvgpr_read_b32 v22, a92\n\t"
    "v_accvgpr_read_b32 v23, a93\n\t"
    "v_accvgpr_read_b32 v24, a94\n\t"
    "v_accvgpr_read_b32 v25, a95\n\t"
    "v_mul_f32 v22, v20, v22\n\t"
    "v_mul_f32 v23, v20, v23\n\t"
    "v_mul_f32 v24, v20, v24\n\t"
    "v_mul_f32 v25, v20, v25\n\t"
    "v_accvgpr_write_b32 a92, v22\n\t"
    "v_accvgpr_write_b32 a93, v23\n\t"
    "v_accvgpr_write_b32 a94, v24\n\t"
    "v_accvgpr_write_b32 a95, v25\n\t"
    "v_accvgpr_read_b32 v22, a108\n\t"
    "v_accvgpr_read_b32 v23, a109\n\t"
    "v_accvgpr_read_b32 v24, a110\n\t"
    "v_accvgpr_read_b32 v25, a111\n\t"
    "v_mul_f32 v22, v20, v22\n\t"
    "v_mul_f32 v23, v20, v23\n\t"
    "v_mul_f32 v24, v20, v24\n\t"
    "v_mul_f32 v25, v20, v25\n\t"
    "v_accvgpr_write_b32 a108, v22\n\t"
    "v_accvgpr_write_b32 a109, v23\n\t"
    "v_accvgpr_write_b32 a110, v24\n\t"
    "v_accvgpr_write_b32 a111, v25\n\t"
    "v_accvgpr_read_b32 v22, a124\n\t"
    "v_accvgpr_read_b32 v23, a125\n\t"
    "v_accvgpr_read_b32 v24, a126\n\t"
    "v_accvgpr_read_b32 v25, a127\n\t"
    "v_mul_f32 v22, v20, v22\n\t"
    "v_mul_f32 v23, v20, v23\n\t"
    "v_mul_f32 v24, v20, v24\n\t"
    "v_mul_f32 v25, v20, v25\n\t"
    "v_accvgpr_write_b32 a124, v22\n\t"
    "v_accvgpr_write_b32 a125, v23\n\t"
    "v_accvgpr_write_b32 a126, v24\n\t"
    "v_accvgpr_write_b32 a127, v25\n\t"
    "v_exp_f32 v22, v76\n\t"
    "v_exp_f32 v23, v77\n\t"
    "s_nop 0\n\t"
    "v_cvt_pk_bf16_f32 v124, v22, v23\n\t"
    "v_exp_f32 v22, v78\n\t"
    "v_exp_f32 v23, v79\n\t"
    "s_nop 0\n\t"
    "v_cvt_pk_bf16_f32 v125, v22, v23\n\t"
    "v_exp_f32 v22, v92\n\t"
    "v_exp_f32 v23, v93\n\t"
    "s_nop 0\n\t"
    "v_cvt_pk_bf16_f32 v126, v22, v23\n\t"
    "v_exp_f32 v22, v94\n\t"
    "v_exp_f32 v23, v95\n\t"
    "s_nop 0\n\t"
    "v_cvt_pk_bf16_f32 v127, v22, v23\n\t"
    "s_nop 1\n\t"
    "v_mfma_f32_16x16x32_bf16 v[172:175], v[176:179], v[124:127], v[156:159]\n\t"
    "s_nop 7\n\t"
    "s_setpc_b64 s[34:35]\n\t"
    "L_wait0p0_%=:\n\t"
    "s_sleep 1\n\t"
    "s_memrealtime s[38:39]\n\t"
    "s_waitcnt lgkmcnt(0)\n\t"
    "s_sub_u32 s37, s36, s38\n\t"
    "s_cmp_gt_i32 s37, 0\n\t"
    "s_cbranch_scc1 L_wait0p0_%=\n\t"
    "s_branch L_back0p0_%=\n\t"
    "L_wait0p2_%=:\n\t"
    "s_sleep 1\n\t"
    "s_memrealtime s[38:39]\n\t"
    "s_waitcnt lgkmcnt(0)\n\t"
    "s_sub_u32 s37, s36, s38\n\t"
    "s_cmp_gt_i32 s37, 0\n\t"
    "s_cbranch_scc1 L_wait0p2_%=\n\t"
    "s_branch L_back0p2_%=\n\t"
    "L_wait0p4_%=:\n\t"
    "s_sleep 1\n\t"
    "s_memrealtime s[38:39]\n\t"
    "s_waitcnt lgkmcnt(0)\n\t"
    "s_sub_u32 s37, s36, s38\n\t"
    "s_cmp_gt_i32 s37, 0\n\t"
    "s_cbranch_scc1 L_wait0p4_%=\n\t"
    "s_branch L_back0p4_%=\n\t"
    "L_wait0p6_%=:\n\t"
    "s_sleep 1\n\t"
    "s_memrealtime s[38:39]\n\t"
    "s_waitcnt lgkmcnt(0)\n\t"
    "s_sub_u32 s37, s36, s38\n\t"
    "s_cmp_gt_i32 s37, 0\n\t"
    "s_cbranch_scc1 L_wait0p6_%=\n\t"
    "s_branch L_back0p6_%=\n\t"
    "L_wait0_%=:\n\t"
    "s_sleep 1\n\t"
    "s_memrealtime s[38:39]\n\t"
    "s_waitcnt lgkmcnt(0)\n\t"
    "s_sub_u32 s37, s36, s38\n\t"
    "s_cmp_gt_i32 s37, 0\n\t"
    "s_cbranch_scc1 L_wait0_%=\n\t"
    "s_branch L_back0_%=\n\t"
    "L_done_%=:\n\t"
    :: "s"(ka), "s"(bh), "s"(wid), "s"(ldsb)
    : "v0", "v1", "v2", "v3", "v4", "v5", "v6", "v7", "v8", "v9", "v10", "v11", "v12", "v13", "v14", "v15", "v16", "v17", "v18", "v19", "v20", "v21", "v22", "v23", "v24", "v25", "v26", "v27", "v28", "v29", "v30", "v31", "v32", "v33", "v34", "v35", "v36", "v37", "v38", "v39", "v40", "v41", "v42", "v43", "v44", "v45", "v46", "v47", "v48", "v49", "v50", "v51", "v52", "v53", "v54", "v55", "v56", "v57", "v58", "v59", "v60", "v61", "v62", "v63", "v64", "v65", "v66", "v67", "v68", "v69", "v70", "v71", "v72", "v73", "v74", "v75", "v76", "v77", "v78", "v79", "v80", "v81", "v82", "v83", "v84", "v85", "v86", "v87", "v88", "v89", "v90", "v91", "v92", "v93", "v94", "v95", "v96", "v97", "v98", "v99", "v100", "v101", "v102", "v103", "v104", "v105", "v106", "v107", "v108", "v109", "v110", "v111", "v112", "v113", "v114", "v115", "v116", "v117", "v118", "v119", "v120", "v121", "v122", "v123", "v124", "v125", "v126", "v127", "v128", "v129", "v130", "v131", "v132", "v133", "v134", "v135", "v136", "v137", "v138", "v139", "v140", "v141", "v142", "v143", "v144", "v145", "v146", "v147", "v148", "v149", "v150", "v151", "v152", "v153", "v154", "v155", "v156", "v157", "v158", "v159", "v160", "v161", "v162", "v163", "v164", "v165", "v166", "v167", "v168", "v169", "v170", "v171", "v172", "v173", "v174", "v175", "v176", "v177", "v178", "v179", "v180", "v181", "v182", "v183", "v184", "v185", "v186", "v187", "v188", "v189", "v190", "v191", "v192", "v193", "v194", "v195", "v196", "v197", "v198", "v199", "v200", "v201", "v202", "v203", "v204", "v205", "v206", "v207", "v208", "v209", "v210", "v211", "v212", "v213", "v214", "v215", "v216", "v217", "v218", "v219", "v220", "v221", "v222", "v223", "v224", "v225", "v226", "v227", "v228", "v229", "v230", "v231", "v232", "v233", "v234", "v235", "v236", "v237", "v238", "v239", "v240", "v241", "v242", "v243", "v244", "v245", "v246", "v247", "v248", "v249", "v250", "v251", "v252", "v253", "v254", "v255", "a0", "a1", "a2", "a3", "a4", "a5", "a6", "a7", "a8", "a9", "a10", "a11", "a12", "a13", "a14", "a15", "a16", "a17", "a18", "a19", "a20", "a21", "a22", "a23", "a24", "a25", "a26", "a27", "a28", "a29", "a30", "a31", "a32", "a33", "a34", "a35", "a36", "a37", "a38", "a39", "a40", "a41", "a42", "a43", "a44", "a45", "a46", "a47", "a48", "a49", "a50", "a51", "a52", "a53", "a54", "a55", "a56", "a57", "a58", "a59", "a60", "a61", "a62", "a63", "a64", "a65", "a66", "a67", "a68", "a69", "a70", "a71", "a72", "a73", "a74", "a75", "a76", "a77", "a78", "a79", "a80", "a81", "a82", "a83", "a84", "a85", "a86", "a87", "a88", "a89", "a90", "a91", "a92", "a93", "a94", "a95", "a96", "a97", "a98", "a99", "a100", "a101", "a102", "a103", "a104", "a105", "a106", "a107", "a108", "a109", "a110", "a111", "a112", "a113", "a114", "a115", "a116", "a117", "a118", "a119", "a120", "a121", "a122", "a123", "a124", "a125", "a126", "a127", "a128", "a129", "a130", "a131", "a132", "a133", "a134", "a135", "a136", "a137", "a138", "a139", "a140", "a141", "a142", "a143", "a144", "a145", "a146", "a147", "a148", "a149", "a150", "a151", "a152", "a153", "a154", "a155", "a156", "a157", "a158", "a159", "a160", "a161", "a162", "a163", "a164", "a165", "a166", "a167", "a168", "a169", "a170", "a171", "a172", "a173", "a174", "a175", "a176", "a177", "a178", "a179", "a180", "a181", "a182", "a183", "a184", "a185", "a186", "a187", "a188", "a189", "a190", "a191", "a192", "a193", "a194", "a195", "a196", "a197", "a198", "a199", "a200", "a201", "a202", "a203", "a204", "a205", "a206", "a207", "a208", "a209", "a210", "a211", "a212", "a213", "a214", "a215", "a216", "a217", "a218", "a219", "a220", "a221", "a222", "a223", "a224", "a225", "a226", "a227", "a228", "a229", "a230", "a231", "a232", "a233", "a234", "a235", "a236", "a237", "a238", "a239", "a240", "a241", "a242", "a243", "a244", "a245", "a246", "a247", "a248", "a249", "a250", "a251", "a252", "a253", "a254", "a255", "s26", "s27", "s28", "s29", "s30", "s31", "s32", "s33", "s34", "s35", "s36", "s37", "s38", "s39", "s40", "s41", "s42", "s43", "s44", "s45", "s46", "s47", "s48", "s49", "s50", "s51", "s52", "s53", "s54", "s55", "s56", "s57", "s58", "s59", "s60", "s61", "s62", "s63", "s64", "s65", "s66", "s67", "s68", "s69", "s70", "s71", "vcc", "memory");
}
extern "C" void kernel_launch(void* const* d_in, const int* in_sizes, int n_in, void* d_out, int out_size, void* d_ws, size_t ws_size, hipStream_t stream) {
  (void)hipFuncSetAttribute((const void*)attn_fwd_kernel, hipFuncAttributeMaxDynamicSharedMemorySize, 163840);
  hipLaunchKernelGGL(attn_fwd_kernel, dim3(256), dim3(256), 163840, stream, (const char*)d_in[0], (const char*)d_in[1], (const char*)d_in[2], (char*)d_out, (char*)d_ws);
}
```
